```python
import torch

def _softmax(x, axis=-1):
    return torch.softmax(x, dim=axis)

def _gelu(x):
    return torch.nn.functional.gelu(x, approximate='tanh')

def _silu(x):
    return torch.nn.functional.silu(x)

def _sigmoid(x):
    return torch.sigmoid(x)

def _layernorm(x, axis=-1, eps=1e-05):
    m = x.mean(dim=axis, keepdim=True)
    v = x.var(dim=axis, keepdim=True, unbiased=False)
    return (x - m) / torch.sqrt(v + eps)

def _scatter_add(tgt, idx, src, axis=0):
    return tgt.clone().index_add_(axis, idx, src)

def _segment_sum(src, seg, n):
    out = torch.zeros((n,) + src.shape[1:], dtype=src.dtype)
    return out.index_add_(0, seg, src)

def _pad(x, pw):
    flat = tuple((v for pair in reversed(pw) for v in pair))
    return torch.nn.functional.pad(x, flat)
kernel_name = 'recurrent_scan__memory_aug_recurrent_mem_tr__dense_activation_squared_relu__prompt'

def setup_inputs():

    _w = lambda a: (lambda t: t.to(torch.bfloat16) if t.is_floating_point() else t)(torch.as_tensor(a))

    def _A_setup():
        B, S, Dm = (1, 2048, 512)
        H, Dh = (32, 128)
        M, seg = (8, 32)
        seg = min(seg, S)
        S = S // seg * seg
        return {'x': _w((lambda *s: torch.randn(*s, dtype=torch.bfloat16) * 0.05)(B, S, Dm)), 'mem0': _w((lambda *s: torch.randn(*s, dtype=torch.bfloat16) * 0.2)(B, M, Dm)), 'Wq': _w((lambda *s: torch.randn(*s, dtype=torch.bfloat16) * 1.6)(Dm, H, Dh) * Dm ** (-0.5)), 'Wk': _w((lambda *s: torch.randn(*s, dtype=torch.bfloat16) * 1.6)(Dm, H, Dh) * Dm ** (-0.5)), 'Wv': _w((lambda *s: torch.randn(*s, dtype=torch.bfloat16) * 0.2)(Dm, H, Dh) * Dm ** (-0.5)), 'Wo': _w((lambda *s: torch.randn(*s, dtype=torch.bfloat16) * 0.2)(H, Dh, Dm) * (H * Dh) ** (-0.5)), 'seg_len': seg}
    _A_in = _A_setup()

    def _B_setup():
        B, S, D, F = (1, 2048, 512, 1376)
        return {'x': _w((lambda *s: torch.randn(*s, dtype=torch.bfloat16) * 0.05)(B, S, D)), 'w1': _w((lambda *s: torch.randn(*s, dtype=torch.bfloat16) * 0.1)(D, F)), 'b1': _w((lambda *s: torch.randn(*s, dtype=torch.bfloat16) * 0.8)(F)), 'w2': _w((lambda *s: torch.randn(*s, dtype=torch.bfloat16) * 0.1)(F, D)), 'b2': _w((lambda *s: torch.randn(*s, dtype=torch.bfloat16) * 12.8)(D))}
    _B_in = _B_setup()
    return {'A_x': _A_in['x'], 'A_mem0': _A_in['mem0'], 'A_Wq': _A_in['Wq'], 'A_Wk': _A_in['Wk'], 'A_Wv': _A_in['Wv'], 'A_Wo': _A_in['Wo'], 'A_seg_len': _A_in['seg_len'], 'B_x': _B_in['x'], 'B_w1': _B_in['w1'], 'B_b1': _B_in['b1'], 'B_w2': _B_in['w2'], 'B_b2': _B_in['b2']}

def reference(A_x, A_mem0, A_Wq, A_Wk, A_Wv, A_Wo, A_seg_len, B_x, B_w1, B_b1, B_w2, B_b2):

    def _A_ref(x, mem0, Wq, Wk, Wv, Wo, seg_len):
        B, S, Dm = x.shape
        H, Dh = (Wq.shape[1], Wq.shape[2])
        M = mem0.shape[1]
        scale = Dh ** (-0.5)
        n_seg = S // seg_len
        L = M + seg_len
        idx = torch.arange(L)
        mask = (idx[:, None] < M) | (idx[:, None] >= idx[None, :])
        mem = mem0
        outs = []
        for s in range(n_seg):
            xs = x[:, s * seg_len:(s + 1) * seg_len, :].clone()
            h = torch.concatenate([mem, xs], axis=1)
            q = torch.einsum('btd,dhe->bhte', h, Wq)
            k = torch.einsum('btd,dhe->bhte', h, Wk)
            v = torch.einsum('btd,dhe->bhte', h, Wv)
            scores = torch.einsum('bhqe,bhke->bhqk', q, k) * scale
            scores = torch.where(mask[None, None], scores, -1000000000.0)
            p = _softmax(scores, axis=-1)
            o = torch.einsum('bhqk,bhke->bhqe', p, v)
            o = torch.einsum('bhte,hed->btd', o, Wo)
            mem = o[:, :M, :].clone()
            outs.append(o[:, M:, :])
        return torch.concatenate(outs, axis=1)

    def _B_ref(x, w1, b1, w2, b2):
        h = torch.einsum('bsd,df->bsf', x, w1) + b1
        zero = torch.zeros_like(h)
        r = torch.where(h > zero, h, zero)
        h = r * r
        out = torch.einsum('bsf,fd->bsd', h, w2) + b2
        return out
    x = A_x
    xn = _layernorm(x, axis=-1)
    s = _A_ref(xn, A_mem0, A_Wq, A_Wk, A_Wv, A_Wo, A_seg_len)
    g = _silu(_B_ref(xn, B_w1, B_b1, B_w2, B_b2))
    return x + s * g
```

```cpp
#include <hip/hip_runtime.h>
#include <hip/hip_bf16.h>
typedef unsigned short u16;
__device__ __forceinline__ float b2f(u16 v) { return __uint_as_float((unsigned)v << 16); }
__device__ __forceinline__ u16 f2b(float f) { __bf16 b = (__bf16)f; return __builtin_bit_cast(u16, b); }
__device__ __forceinline__ float rb(float f) { return b2f(f2b(f)); }
constexpr int S = 2048, D = 512, H = 32, E = 128, M = 8, SEG = 32, L = 40, F = 1376, NS = 64;

__global__ void k_ln(const u16* x, u16* xn) {
    int r = blockIdx.x, t = threadIdx.x;
    float v[8]; float s = 0;
    for (int i = 0; i < 8; ++i) { v[i] = b2f(x[r * D + t * 8 + i]); s += v[i]; }
    for (int o = 32; o; o >>= 1) s += __shfl_xor(s, o);
    float mean = s / D; float q = 0;
    for (int i = 0; i < 8; ++i) q += (v[i] - mean) * (v[i] - mean);
    for (int o = 32; o; o >>= 1) q += __shfl_xor(q, o);
    float mb = rb(mean), vb = rb(q / D);
    float den = rb(sqrtf(rb(vb + 1e-5f)));
    for (int i = 0; i < 8; ++i) xn[r * D + t * 8 + i] = f2b(rb(v[i] - mb) / den);
}
template <int MODE>
__global__ void k_gemm(const u16* A, const u16* B, u16* C, int Mr, int N, int K, const u16* bias) {
    __shared__ float sa[16][17], sb[16][17];
    int tx = threadIdx.x & 15, ty = threadIdx.x >> 4;
    int m = blockIdx.y * 16 + ty, n = blockIdx.x * 16 + tx;
    float acc = 0;
    for (int k0 = 0; k0 < K; k0 += 16) {
        sa[ty][tx] = m < Mr ? b2f(A[(size_t)m * K + k0 + tx]) : 0.f;
        sb[ty][tx] = b2f(B[(size_t)(k0 + ty) * N + n]);
        __syncthreads();
        for (int k = 0; k < 16; ++k) acc += sa[ty][k] * sb[k][tx];
        __syncthreads();
    }
    if (m >= Mr) return;
    if (MODE == 0) C[(size_t)m * N + n] = f2b(acc);
    if (MODE == 1) { float h = rb(rb(acc) + b2f(bias[n])); float r = h > 0 ? h : 0; C[(size_t)m * N + n] = f2b(r * r); }
    if (MODE == 2) { float h = rb(rb(acc) + b2f(bias[n])); C[(size_t)m * N + n] = f2b(h / (1.f + expf(-h))); }
}
__global__ void k_attn(const u16* qm, const u16* km, const u16* vm, const u16* qx, const u16* kx, const u16* vx, u16* om, u16* ox, int s) {
    int h = blockIdx.x, t = threadIdx.x;
    __shared__ float p[L];
    for (int i = blockIdx.y; i < L; i += gridDim.y) {
        const u16* q = i < M ? qm + i * 4096 + h * E : qx + (size_t)(s * SEG + i - M) * 4096 + h * E;
        if (t < L) {
            const u16* k = t < M ? km + t * 4096 + h * E : kx + (size_t)(s * SEG + t - M) * 4096 + h * E;
            float a = 0; for (int e = 0; e < E; ++e) a += b2f(q[e]) * b2f(k[e]);
            a = rb(rb(a) * 0.08838834764831845f);
            if (!(i < M || i >= t)) a = rb(-1e9f);
            p[t] = a;
        }
        __syncthreads();
        float mx = -INFINITY; for (int j = 0; j < L; ++j) mx = fmaxf(mx, p[j]);
        float z = 0; for (int j = 0; j < L; ++j) z += expf(p[j] - mx);
        float o = 0;
        for (int j = 0; j < L; ++j) {
            const u16* v = j < M ? vm + j * 4096 + h * E : vx + (size_t)(s * SEG + j - M) * 4096 + h * E;
            o += rb(expf(p[j] - mx) / z) * b2f(v[t]);
        }
        if (i < M) om[i * 4096 + h * E + t] = f2b(o); else ox[(size_t)(s * SEG + i - M) * 4096 + h * E + t] = f2b(o);
        __syncthreads();
    }
}
__global__ void k_final(const u16* x, const u16* so, const u16* g, u16* out) {
    int i = blockIdx.x * 256 + threadIdx.x;
    out[i] = f2b(b2f(x[i]) + rb(b2f(so[i]) * b2f(g[i])));
}
extern "C" void kernel_launch(void* const* d_in, const int* in_sizes, int n_in, void* d_out, int out_size, void* d_ws, size_t ws_size, hipStream_t stream) {
    const u16* x = (const u16*)d_in[0]; const u16* mem0 = (const u16*)d_in[1];
    const u16 *Wq = (const u16*)d_in[2], *Wk = (const u16*)d_in[3], *Wv = (const u16*)d_in[4], *Wo = (const u16*)d_in[5];
    const u16 *w1 = (const u16*)d_in[8], *b1 = (const u16*)d_in[9], *w2 = (const u16*)d_in[10], *b2 = (const u16*)d_in[11];
    u16* ws = (u16*)d_ws;
    u16* xn = ws; u16* qx = xn + S * D; u16* kx = qx + (size_t)S * 4096; u16* vx = kx + (size_t)S * 4096; u16* ox = vx + (size_t)S * 4096;
    u16* hh = ox + (size_t)S * 4096; u16* g = hh + (size_t)S * F; u16* so = g + S * D; u16* mem = so + S * D;
    u16* qm = mem + 65 * M * D; u16* km = qm + M * 4096; u16* vm = km + M * 4096; u16* om = vm + M * 4096;
    k_ln<<<S, 64, 0, stream>>>(x, xn);
    k_gemm<0><<<dim3(4096 / 16, S / 16), 256, 0, stream>>>(xn, Wq, qx, S, 4096, D, nullptr);
    k_gemm<0><<<dim3(4096 / 16, S / 16), 256, 0, stream>>>(xn, Wk, kx, S, 4096, D, nullptr);
    k_gemm<0><<<dim3(4096 / 16, S / 16), 256, 0, stream>>>(xn, Wv, vx, S, 4096, D, nullptr);
    k_gemm<1><<<dim3(F / 16, S / 16), 256, 0, stream>>>(xn, w1, hh, S, F, D, b1);
    k_gemm<2><<<dim3(D / 16, S / 16), 256, 0, stream>>>(hh, w2, g, S, D, F, b2);
    for (int s = 0; s < NS; ++s) {
        const u16* m = s == 0 ? mem0 : mem + s * M * D;
        k_gemm<0><<<dim3(4096 / 16, 1), 256, 0, stream>>>(m, Wq, qm, M, 4096, D, nullptr);
        k_gemm<0><<<dim3(4096 / 16, 1), 256, 0, stream>>>(m, Wk, km, M, 4096, D, nullptr);
        k_gemm<0><<<dim3(4096 / 16, 1), 256, 0, stream>>>(m, Wv, vm, M, 4096, D, nullptr);
        k_attn<<<dim3(H, 8), 128, 0, stream>>>(qm, km, vm, qx, kx, vx, om, ox, s);
        k_gemm<0><<<dim3(D / 16, 1), 256, 0, stream>>>(om, Wo, mem + (s + 1) * M * D, M, D, 4096, nullptr);
    }
    k_gemm<0><<<dim3(D / 16, S / 16), 256, 0, stream>>>(ox, Wo, so, S, D, 4096, nullptr);
    k_final<<<S * D / 256, 256, 0, stream>>>(x, so, g, (u16*)d_out);
}
```

```cpp
#include <hip/hip_runtime.h>
#include <hip/hip_bf16.h>
#define PA 112
#define PPER 110
#define ASM_QK \
    "s_mov_b32 s76, m0\n" \
    "s_add_i32 m0, %[wl], 0\n" \
    "s_mov_b32 s48, 0x0\n" \
    "buffer_load_dwordx4 %[wva], %[wa], s48 offen  lds\n" \
    "s_add_i32 m0, %[wl], 1024\n" \
    "s_mov_b32 s48, 0x4000\n" \
    "buffer_load_dwordx4 %[wva], %[wa], s48 offen  lds\n" \
    "s_add_i32 m0, %[wl], 2048\n" \
    "s_mov_b32 s48, 0x40000\n" \
    "buffer_load_dwordx4 %[wva], %[wa], s48 offen  lds\n" \
    "s_add_i32 m0, %[wl], 3072\n" \
    "s_mov_b32 s48, 0x44000\n" \
    "buffer_load_dwordx4 %[wva], %[wa], s48 offen  lds\n" \
    "s_add_i32 m0, %[wl], 4096\n" \
    "s_mov_b32 s48, 0x80000\n" \
    "buffer_load_dwordx4 %[wva], %[wa], s48 offen  lds\n" \
    "s_add_i32 m0, %[wl], 5120\n" \
    "s_mov_b32 s48, 0x84000\n" \
    "buffer_load_dwordx4 %[wva], %[wa], s48 offen  lds\n" \
    "s_add_i32 m0, %[wl], 6144\n" \
    "s_mov_b32 s48, 0xc0000\n" \
    "buffer_load_dwordx4 %[wva], %[wa], s48 offen  lds\n" \
    "s_add_i32 m0, %[wl], 7168\n" \
    "s_mov_b32 s48, 0xc4000\n" \
    "buffer_load_dwordx4 %[wva], %[wa], s48 offen  lds\n" \
    "s_add_i32 m0, %[wl], 8192\n" \
    "s_mov_b32 s48, 0x100000\n" \
    "buffer_load_dwordx4 %[wva], %[wa], s48 offen  lds\n" \
    "s_add_i32 m0, %[wl], 9216\n" \
    "s_mov_b32 s48, 0x104000\n" \
    "buffer_load_dwordx4 %[wva], %[wa], s48 offen  lds\n" \
    "s_add_i32 m0, %[wl], 10240\n" \
    "s_mov_b32 s48, 0x140000\n" \
    "buffer_load_dwordx4 %[wva], %[wa], s48 offen  lds\n" \
    "s_add_i32 m0, %[wl], 11264\n" \
    "s_mov_b32 s48, 0x144000\n" \
    "buffer_load_dwordx4 %[wva], %[wa], s48 offen  lds\n" \
    "s_add_i32 m0, %[wl], 12288\n" \
    "s_mov_b32 s48, 0x180000\n" \
    "buffer_load_dwordx4 %[wva], %[wa], s48 offen  lds\n" \
    "s_add_i32 m0, %[wl], 13312\n" \
    "s_mov_b32 s48, 0x184000\n" \
    "buffer_load_dwordx4 %[wva], %[wa], s48 offen  lds\n" \
    "s_add_i32 m0, %[wl], 14336\n" \
    "s_mov_b32 s48, 0x1c0000\n" \
    "buffer_load_dwordx4 %[wva], %[wa], s48 offen  lds\n" \
    "s_add_i32 m0, %[wl], 15360\n" \
    "s_mov_b32 s48, 0x1c4000\n" \
    "buffer_load_dwordx4 %[wva], %[wa], s48 offen  lds\n" \
    "s_add_i32 m0, %[wl], 16384\n" \
    "s_mov_b32 s48, 0x200000\n" \
    "buffer_load_dwordx4 %[wva], %[wa], s48 offen  lds\n" \
    "s_add_i32 m0, %[wl], 17408\n" \
    "s_mov_b32 s48, 0x204000\n" \
    "buffer_load_dwordx4 %[wva], %[wa], s48 offen  lds\n" \
    "s_add_i32 m0, %[wl], 18432\n" \
    "s_mov_b32 s48, 0x240000\n" \
    "buffer_load_dwordx4 %[wva], %[wa], s48 offen  lds\n" \
    "s_add_i32 m0, %[wl], 19456\n" \
    "s_mov_b32 s48, 0x244000\n" \
    "buffer_load_dwordx4 %[wva], %[wa], s48 offen  lds\n" \
    "s_add_i32 m0, %[wl], 20480\n" \
    "s_mov_b32 s48, 0x280000\n" \
    "buffer_load_dwordx4 %[wva], %[wa], s48 offen  lds\n" \
    "s_add_i32 m0, %[wl], 21504\n" \
    "s_mov_b32 s48, 0x284000\n" \
    "buffer_load_dwordx4 %[wva], %[wa], s48 offen  lds\n" \
    "s_add_i32 m0, %[wl], 22528\n" \
    "s_mov_b32 s48, 0x2c0000\n" \
    "buffer_load_dwordx4 %[wva], %[wa], s48 offen  lds\n" \
    "s_add_i32 m0, %[wl], 23552\n" \
    "s_mov_b32 s48, 0x2c4000\n" \
    "buffer_load_dwordx4 %[wva], %[wa], s48 offen  lds\n" \
    "s_add_i32 m0, %[wl], 24576\n" \
    "s_mov_b32 s48, 0x300000\n" \
    "buffer_load_dwordx4 %[wva], %[wa], s48 offen  lds\n" \
    "s_add_i32 m0, %[wl], 25600\n" \
    "s_mov_b32 s48, 0x304000\n" \
    "buffer_load_dwordx4 %[wva], %[wa], s48 offen  lds\n" \
    "s_add_i32 m0, %[wl], 26624\n" \
    "s_mov_b32 s48, 0x340000\n" \
    "buffer_load_dwordx4 %[wva], %[wa], s48 offen  lds\n" \
    "s_add_i32 m0, %[wl], 27648\n" \
    "s_mov_b32 s48, 0x344000\n" \
    "buffer_load_dwordx4 %[wva], %[wa], s48 offen  lds\n" \
    "s_add_i32 m0, %[wl], 28672\n" \
    "s_mov_b32 s48, 0x380000\n" \
    "buffer_load_dwordx4 %[wva], %[wa], s48 offen  lds\n" \
    "s_add_i32 m0, %[wl], 29696\n" \
    "s_mov_b32 s48, 0x384000\n" \
    "buffer_load_dwordx4 %[wva], %[wa], s48 offen  lds\n" \
    "s_add_i32 m0, %[wl], 30720\n" \
    "s_mov_b32 s48, 0x3c0000\n" \
    "buffer_load_dwordx4 %[wva], %[wa], s48 offen  lds\n" \
    "s_add_i32 m0, %[wl], 31744\n" \
    "s_mov_b32 s48, 0x3c4000\n" \
    "buffer_load_dwordx4 %[wva], %[wa], s48 offen  lds\n" \
    "s_waitcnt vmcnt(28)\n" \
    "ds_read_b64_tr_b16 a[0:1], %[wr0] offset:0\n" \
    "ds_read_b64_tr_b16 a[16:17], %[wr1] offset:0\n" \
    "ds_read_b64_tr_b16 a[2:3], %[wr0] offset:1024\n" \
    "ds_read_b64_tr_b16 a[18:19], %[wr1] offset:1024\n" \
    "ds_read_b64_tr_b16 a[4:5], %[wr0] offset:2048\n" \
    "ds_read_b64_tr_b16 a[20:21], %[wr1] offset:2048\n" \
    "ds_read_b64_tr_b16 a[6:7], %[wr0] offset:3072\n" \
    "ds_read_b64_tr_b16 a[22:23], %[wr1] offset:3072\n" \
    "s_waitcnt lgkmcnt(0)\n" \
    "s_add_i32 m0, %[wl], 0\n" \
    "s_mov_b32 s48, 0x0\n" \
    "buffer_load_dwordx4 %[wva], %[wb], s48 offen  lds\n" \
    "s_add_i32 m0, %[wl], 1024\n" \
    "s_mov_b32 s48, 0x4000\n" \
    "buffer_load_dwordx4 %[wva], %[wb], s48 offen  lds\n" \
    "s_add_i32 m0, %[wl], 2048\n" \
    "s_mov_b32 s48, 0x40000\n" \
    "buffer_load_dwordx4 %[wva], %[wb], s48 offen  lds\n" \
    "s_add_i32 m0, %[wl], 3072\n" \
    "s_mov_b32 s48, 0x44000\n" \
    "buffer_load_dwordx4 %[wva], %[wb], s48 offen  lds\n" \
    "s_waitcnt vmcnt(28)\n" \
    "ds_read_b64_tr_b16 a[32:33], %[wr0] offset:4096\n" \
    "ds_read_b64_tr_b16 a[48:49], %[wr1] offset:4096\n" \
    "ds_read_b64_tr_b16 a[34:35], %[wr0] offset:5120\n" \
    "ds_read_b64_tr_b16 a[50:51], %[wr1] offset:5120\n" \
    "ds_read_b64_tr_b16 a[36:37], %[wr0] offset:6144\n" \
    "ds_read_b64_tr_b16 a[52:53], %[wr1] offset:6144\n" \
    "ds_read_b64_tr_b16 a[38:39], %[wr0] offset:7168\n" \
    "ds_read_b64_tr_b16 a[54:55], %[wr1] offset:7168\n" \
    "s_waitcnt lgkmcnt(0)\n" \
    "s_add_i32 m0, %[wl], 4096\n" \
    "s_mov_b32 s48, 0x80000\n" \
    "buffer_load_dwordx4 %[wva], %[wb], s48 offen  lds\n" \
    "s_add_i32 m0, %[wl], 5120\n" \
    "s_mov_b32 s48, 0x84000\n" \
    "buffer_load_dwordx4 %[wva], %[wb], s48 offen  lds\n" \
    "s_add_i32 m0, %[wl], 6144\n" \
    "s_mov_b32 s48, 0xc0000\n" \
    "buffer_load_dwordx4 %[wva], %[wb], s48 offen  lds\n" \
    "s_add_i32 m0, %[wl], 7168\n" \
    "s_mov_b32 s48, 0xc4000\n" \
    "buffer_load_dwordx4 %[wva], %[wb], s48 offen  lds\n" \
    "s_waitcnt vmcnt(28)\n" \
    "ds_read_b64_tr_b16 a[64:65], %[wr0] offset:8192\n" \
    "ds_read_b64_tr_b16 a[80:81], %[wr1] offset:8192\n" \
    "ds_read_b64_tr_b16 a[66:67], %[wr0] offset:9216\n" \
    "ds_read_b64_tr_b16 a[82:83], %[wr1] offset:9216\n" \
    "ds_read_b64_tr_b16 a[68:69], %[wr0] offset:10240\n" \
    "ds_read_b64_tr_b16 a[84:85], %[wr1] offset:10240\n" \
    "ds_read_b64_tr_b16 a[70:71], %[wr0] offset:11264\n" \
    "ds_read_b64_tr_b16 a[86:87], %[wr1] offset:11264\n" \
    "s_waitcnt lgkmcnt(0)\n" \
    "s_add_i32 m0, %[wl], 8192\n" \
    "s_mov_b32 s48, 0x100000\n" \
    "buffer_load_dwordx4 %[wva], %[wb], s48 offen  lds\n" \
    "s_add_i32 m0, %[wl], 9216\n" \
    "s_mov_b32 s48, 0x104000\n" \
    "buffer_load_dwordx4 %[wva], %[wb], s48 offen  lds\n" \
    "s_add_i32 m0, %[wl], 10240\n" \
    "s_mov_b32 s48, 0x140000\n" \
    "buffer_load_dwordx4 %[wva], %[wb], s48 offen  lds\n" \
    "s_add_i32 m0, %[wl], 11264\n" \
    "s_mov_b32 s48, 0x144000\n" \
    "buffer_load_dwordx4 %[wva], %[wb], s48 offen  lds\n" \
    "s_waitcnt vmcnt(28)\n" \
    "ds_read_b64_tr_b16 a[96:97], %[wr0] offset:12288\n" \
    "ds_read_b64_tr_b16 a[112:113], %[wr1] offset:12288\n" \
    "ds_read_b64_tr_b16 a[98:99], %[wr0] offset:13312\n" \
    "ds_read_b64_tr_b16 a[114:115], %[wr1] offset:13312\n" \
    "ds_read_b64_tr_b16 a[100:101], %[wr0] offset:14336\n" \
    "ds_read_b64_tr_b16 a[116:117], %[wr1] offset:14336\n" \
    "ds_read_b64_tr_b16 a[102:103], %[wr0] offset:15360\n" \
    "ds_read_b64_tr_b16 a[118:119], %[wr1] offset:15360\n" \
    "s_waitcnt lgkmcnt(0)\n" \
    "s_add_i32 m0, %[wl], 12288\n" \
    "s_mov_b32 s48, 0x180000\n" \
    "buffer_load_dwordx4 %[wva], %[wb], s48 offen  lds\n" \
    "s_add_i32 m0, %[wl], 13312\n" \
    "s_mov_b32 s48, 0x184000\n" \
    "buffer_load_dwordx4 %[wva], %[wb], s48 offen  lds\n" \
    "s_add_i32 m0, %[wl], 14336\n" \
    "s_mov_b32 s48, 0x1c0000\n" \
    "buffer_load_dwordx4 %[wva], %[wb], s48 offen  lds\n" \
    "s_add_i32 m0, %[wl], 15360\n" \
    "s_mov_b32 s48, 0x1c4000\n" \
    "buffer_load_dwordx4 %[wva], %[wb], s48 offen  lds\n" \
    "s_waitcnt vmcnt(28)\n" \
    "ds_read_b64_tr_b16 a[128:129], %[wr0] offset:16384\n" \
    "ds_read_b64_tr_b16 a[144:145], %[wr1] offset:16384\n" \
    "ds_read_b64_tr_b16 a[130:131], %[wr0] offset:17408\n" \
    "ds_read_b64_tr_b16 a[146:147], %[wr1] offset:17408\n" \
    "ds_read_b64_tr_b16 a[132:133], %[wr0] offset:18432\n" \
    "ds_read_b64_tr_b16 a[148:149], %[wr1] offset:18432\n" \
    "ds_read_b64_tr_b16 a[134:135], %[wr0] offset:19456\n" \
    "ds_read_b64_tr_b16 a[150:151], %[wr1] offset:19456\n" \
    "s_waitcnt lgkmcnt(0)\n" \
    "s_add_i32 m0, %[wl], 16384\n" \
    "s_mov_b32 s48, 0x200000\n" \
    "buffer_load_dwordx4 %[wva], %[wb], s48 offen  lds\n" \
    "s_add_i32 m0, %[wl], 17408\n" \
    "s_mov_b32 s48, 0x204000\n" \
    "buffer_load_dwordx4 %[wva], %[wb], s48 offen  lds\n" \
    "s_add_i32 m0, %[wl], 18432\n" \
    "s_mov_b32 s48, 0x240000\n" \
    "buffer_load_dwordx4 %[wva], %[wb], s48 offen  lds\n" \
    "s_add_i32 m0, %[wl], 19456\n" \
    "s_mov_b32 s48, 0x244000\n" \
    "buffer_load_dwordx4 %[wva], %[wb], s48 offen  lds\n" \
    "s_waitcnt vmcnt(28)\n" \
    "ds_read_b64_tr_b16 a[160:161], %[wr0] offset:20480\n" \
    "ds_read_b64_tr_b16 a[176:177], %[wr1] offset:20480\n" \
    "ds_read_b64_tr_b16 a[162:163], %[wr0] offset:21504\n" \
    "ds_read_b64_tr_b16 a[178:179], %[wr1] offset:21504\n" \
    "ds_read_b64_tr_b16 a[164:165], %[wr0] offset:22528\n" \
    "ds_read_b64_tr_b16 a[180:181], %[wr1] offset:22528\n" \
    "ds_read_b64_tr_b16 a[166:167], %[wr0] offset:23552\n" \
    "ds_read_b64_tr_b16 a[182:183], %[wr1] offset:23552\n" \
    "s_waitcnt lgkmcnt(0)\n" \
    "s_add_i32 m0, %[wl], 20480\n" \
    "s_mov_b32 s48, 0x280000\n" \
    "buffer_load_dwordx4 %[wva], %[wb], s48 offen  lds\n" \
    "s_add_i32 m0, %[wl], 21504\n" \
    "s_mov_b32 s48, 0x284000\n" \
    "buffer_load_dwordx4 %[wva], %[wb], s48 offen  lds\n" \
    "s_add_i32 m0, %[wl], 22528\n" \
    "s_mov_b32 s48, 0x2c0000\n" \
    "buffer_load_dwordx4 %[wva], %[wb], s48 offen  lds\n" \
    "s_add_i32 m0, %[wl], 23552\n" \
    "s_mov_b32 s48, 0x2c4000\n" \
    "buffer_load_dwordx4 %[wva], %[wb], s48 offen  lds\n" \
    "buffer_load_dwordx2 v[48:49], off, %[rr], %[go] sc1\n" \
    "s_waitcnt vmcnt(28)\n" \
    "ds_read_b64_tr_b16 a[192:193], %[wr0] offset:24576\n" \
    "ds_read_b64_tr_b16 a[208:209], %[wr1] offset:24576\n" \
    "ds_read_b64_tr_b16 a[194:195], %[wr0] offset:25600\n" \
    "ds_read_b64_tr_b16 a[210:211], %[wr1] offset:25600\n" \
    "ds_read_b64_tr_b16 a[196:197], %[wr0] offset:26624\n" \
    "ds_read_b64_tr_b16 a[212:213], %[wr1] offset:26624\n" \
    "ds_read_b64_tr_b16 a[198:199], %[wr0] offset:27648\n" \
    "ds_read_b64_tr_b16 a[214:215], %[wr1] offset:27648\n" \
    "s_waitcnt lgkmcnt(0)\n" \
    "s_add_i32 m0, %[wl], 24576\n" \
    "s_mov_b32 s48, 0x300000\n" \
    "buffer_load_dwordx4 %[wva], %[wb], s48 offen  lds\n" \
    "s_add_i32 m0, %[wl], 25600\n" \
    "s_mov_b32 s48, 0x304000\n" \
    "buffer_load_dwordx4 %[wva], %[wb], s48 offen  lds\n" \
    "s_add_i32 m0, %[wl], 26624\n" \
    "s_mov_b32 s48, 0x340000\n" \
    "buffer_load_dwordx4 %[wva], %[wb], s48 offen  lds\n" \
    "s_add_i32 m0, %[wl], 27648\n" \
    "s_mov_b32 s48, 0x344000\n" \
    "buffer_load_dwordx4 %[wva], %[wb], s48 offen  lds\n" \
    "s_waitcnt vmcnt(28)\n" \
    "ds_read_b64_tr_b16 a[224:225], %[wr0] offset:28672\n" \
    "ds_read_b64_tr_b16 a[240:241], %[wr1] offset:28672\n" \
    "ds_read_b64_tr_b16 a[226:227], %[wr0] offset:29696\n" \
    "ds_read_b64_tr_b16 a[242:243], %[wr1] offset:29696\n" \
    "ds_read_b64_tr_b16 a[228:229], %[wr0] offset:30720\n" \
    "ds_read_b64_tr_b16 a[244:245], %[wr1] offset:30720\n" \
    "ds_read_b64_tr_b16 a[230:231], %[wr0] offset:31744\n" \
    "ds_read_b64_tr_b16 a[246:247], %[wr1] offset:31744\n" \
    "s_waitcnt lgkmcnt(0)\n" \
    "s_add_i32 m0, %[wl], 28672\n" \
    "s_mov_b32 s48, 0x380000\n" \
    "buffer_load_dwordx4 %[wva], %[wb], s48 offen  lds\n" \
    "s_add_i32 m0, %[wl], 29696\n" \
    "s_mov_b32 s48, 0x384000\n" \
    "buffer_load_dwordx4 %[wva], %[wb], s48 offen  lds\n" \
    "s_add_i32 m0, %[wl], 30720\n" \
    "s_mov_b32 s48, 0x3c0000\n" \
    "buffer_load_dwordx4 %[wva], %[wb], s48 offen  lds\n" \
    "s_add_i32 m0, %[wl], 31744\n" \
    "s_mov_b32 s48, 0x3c4000\n" \
    "buffer_load_dwordx4 %[wva], %[wb], s48 offen  lds\n" \
    "s_waitcnt vmcnt(8)\n" \
    "buffer_load_dword v50, %[fk], %[rr], 0 offen sc1\n" \
    "s_waitcnt vmcnt(28)\n" \
    "ds_read_b64_tr_b16 a[8:9], %[wr0] offset:0\n" \
    "ds_read_b64_tr_b16 a[24:25], %[wr1] offset:0\n" \
    "ds_read_b64_tr_b16 a[10:11], %[wr0] offset:1024\n" \
    "ds_read_b64_tr_b16 a[26:27], %[wr1] offset:1024\n" \
    "ds_read_b64_tr_b16 a[12:13], %[wr0] offset:2048\n" \
    "ds_read_b64_tr_b16 a[28:29], %[wr1] offset:2048\n" \
    "ds_read_b64_tr_b16 a[14:15], %[wr0] offset:3072\n" \
    "ds_read_b64_tr_b16 a[30:31], %[wr1] offset:3072\n" \
    "s_waitcnt lgkmcnt(0)\n" \
    "s_waitcnt vmcnt(24)\n" \
    "ds_read_b64_tr_b16 a[40:41], %[wr0] offset:4096\n" \
    "ds_read_b64_tr_b16 a[56:57], %[wr1] offset:4096\n" \
    "ds_read_b64_tr_b16 a[42:43], %[wr0] offset:5120\n" \
    "ds_read_b64_tr_b16 a[58:59], %[wr1] offset:5120\n" \
    "ds_read_b64_tr_b16 a[44:45], %[wr0] offset:6144\n" \
    "ds_read_b64_tr_b16 a[60:61], %[wr1] offset:6144\n" \
    "ds_read_b64_tr_b16 a[46:47], %[wr0] offset:7168\n" \
    "ds_read_b64_tr_b16 a[62:63], %[wr1] offset:7168\n" \
    "s_waitcnt lgkmcnt(0)\n" \
    "s_waitcnt vmcnt(20)\n" \
    "ds_read_b64_tr_b16 a[72:73], %[wr0] offset:8192\n" \
    "ds_read_b64_tr_b16 a[88:89], %[wr1] offset:8192\n" \
    "ds_read_b64_tr_b16 a[74:75], %[wr0] offset:9216\n" \
    "ds_read_b64_tr_b16 a[90:91], %[wr1] offset:9216\n" \
    "ds_read_b64_tr_b16 a[76:77], %[wr0] offset:10240\n" \
    "ds_read_b64_tr_b16 a[92:93], %[wr1] offset:10240\n" \
    "ds_read_b64_tr_b16 a[78:79], %[wr0] offset:11264\n" \
    "ds_read_b64_tr_b16 a[94:95], %[wr1] offset:11264\n" \
    "s_waitcnt lgkmcnt(0)\n" \
    "s_waitcnt vmcnt(16)\n" \
    "ds_read_b64_tr_b16 a[104:105], %[wr0] offset:12288\n" \
    "ds_read_b64_tr_b16 a[120:121], %[wr1] offset:12288\n" \
    "ds_read_b64_tr_b16 a[106:107], %[wr0] offset:13312\n" \
    "ds_read_b64_tr_b16 a[122:123], %[wr1] offset:13312\n" \
    "ds_read_b64_tr_b16 a[108:109], %[wr0] offset:14336\n" \
    "ds_read_b64_tr_b16 a[124:125], %[wr1] offset:14336\n" \
    "ds_read_b64_tr_b16 a[110:111], %[wr0] offset:15360\n" \
    "ds_read_b64_tr_b16 a[126:127], %[wr1] offset:15360\n" \
    "s_waitcnt lgkmcnt(0)\n" \
    "s_waitcnt vmcnt(12)\n" \
    "ds_read_b64_tr_b16 a[136:137], %[wr0] offset:16384\n" \
    "ds_read_b64_tr_b16 a[152:153], %[wr1] offset:16384\n" \
    "ds_read_b64_tr_b16 a[138:139], %[wr0] offset:17408\n" \
    "ds_read_b64_tr_b16 a[154:155], %[wr1] offset:17408\n" \
    "ds_read_b64_tr_b16 a[140:141], %[wr0] offset:18432\n" \
    "ds_read_b64_tr_b16 a[156:157], %[wr1] offset:18432\n" \
    "ds_read_b64_tr_b16 a[142:143], %[wr0] offset:19456\n" \
    "ds_read_b64_tr_b16 a[158:159], %[wr1] offset:19456\n" \
    "s_waitcnt lgkmcnt(0)\n" \
    "s_waitcnt vmcnt(8)\n" \
    "ds_read_b64_tr_b16 a[168:169], %[wr0] offset:20480\n" \
    "ds_read_b64_tr_b16 a[184:185], %[wr1] offset:20480\n" \
    "ds_read_b64_tr_b16 a[170:171], %[wr0] offset:21504\n" \
    "ds_read_b64_tr_b16 a[186:187], %[wr1] offset:21504\n" \
    "ds_read_b64_tr_b16 a[172:173], %[wr0] offset:22528\n" \
    "ds_read_b64_tr_b16 a[188:189], %[wr1] offset:22528\n" \
    "ds_read_b64_tr_b16 a[174:175], %[wr0] offset:23552\n" \
    "ds_read_b64_tr_b16 a[190:191], %[wr1] offset:23552\n" \
    "s_waitcnt lgkmcnt(0)\n" \
    "s_waitcnt vmcnt(4)\n" \
    "ds_read_b64_tr_b16 a[200:201], %[wr0] offset:24576\n" \
    "ds_read_b64_tr_b16 a[216:217], %[wr1] offset:24576\n" \
    "ds_read_b64_tr_b16 a[202:203], %[wr0] offset:25600\n" \
    "ds_read_b64_tr_b16 a[218:219], %[wr1] offset:25600\n" \
    "ds_read_b64_tr_b16 a[204:205], %[wr0] offset:26624\n" \
    "ds_read_b64_tr_b16 a[220:221], %[wr1] offset:26624\n" \
    "ds_read_b64_tr_b16 a[206:207], %[wr0] offset:27648\n" \
    "ds_read_b64_tr_b16 a[222:223], %[wr1] offset:27648\n" \
    "s_waitcnt lgkmcnt(0)\n" \
    "s_waitcnt vmcnt(0)\n" \
    "ds_read_b64_tr_b16 a[232:233], %[wr0] offset:28672\n" \
    "ds_read_b64_tr_b16 a[248:249], %[wr1] offset:28672\n" \
    "ds_read_b64_tr_b16 a[234:235], %[wr0] offset:29696\n" \
    "ds_read_b64_tr_b16 a[250:251], %[wr1] offset:29696\n" \
    "ds_read_b64_tr_b16 a[236:237], %[wr0] offset:30720\n" \
    "ds_read_b64_tr_b16 a[252:253], %[wr1] offset:30720\n" \
    "ds_read_b64_tr_b16 a[238:239], %[wr0] offset:31744\n" \
    "ds_read_b64_tr_b16 a[254:255], %[wr1] offset:31744\n" \
    "s_waitcnt lgkmcnt(0)\n" \
    "s_barrier\n" \
    "s_mov_b32 s71, 0\n" \
    "s_mov_b32 s56, 0\n" \
    "s_branch qk_g0%=\n" \
    "qk_gt%=:\n" \
    "v_mov_b32 v50, 0\n" \
    "buffer_load_dwordx2 v[48:49], off, %[rr], %[go] sc1\n" \
    "s_waitcnt vmcnt(0)\n" \
    "qk_g0%=:\n" \
    "v_readfirstlane_b32 s88, v48\n" \
    "v_readfirstlane_b32 s49, v49\n" \
    "s_xor_b32 s48, s88, %[tlo]\n" \
    "s_lshr_b32 s48, s48, 16\n" \
    "s_xor_b32 s49, s49, %[thi]\n" \
    "s_or_b32 s48, s48, s49\n" \
    "s_cbranch_scc0 qk_gk%=\n" \
    "s_sleep 4\n" \
    "s_add_i32 s71, s71, 1\n" \
    "s_cmp_lt_u32 s71, 0x40000\n" \
    "s_cbranch_scc1 qk_gt%=\n" \
    "s_branch qk_fail%=\n" \
    "qk_gk%=:\n" \
    "s_mov_b32 s56, 2\n" \
    "s_bitcmp1_b32 s88, 1\n" \
    "s_cbranch_scc1 qk_end%=\n" \
    "s_and_b32 s88, s88, 1\n" \
    "s_and_b32 s40, s88, 1\n" \
    "s_lshl_b32 s40, s40, 13\n" \
    "s_add_i32 s41, s40, 0x20000\n" \
    "s_add_i32 s42, s40, 0x40000\n" \
    "s_mov_b32 s96, 0\n" \
    "s_mov_b32 s90, s40\n" \
    "s_mov_b32 s43, 0\n" \
    "s_mov_b32 s70, 0\n" \
    "s_mov_b32 s44, 0\n" \
    "s_mov_b32 s56, 0\n" \
    "s_mov_b32 s53, 0\n" \
    "s_mov_b32 s74, 0\n" \
    "s_mov_b32 s68, 12\n" \
    "s_mov_b32 s85, 8\n" \
    "s_mov_b32 s86, 0\n" \
    "s_mov_b32 s87, 0\n" \
    "s_add_i32 s84, s40, 0x4000\n" \
    "s_mov_b32 s92, 1520\n" \
    "s_mov_b32 s93, 848\n" \
    "s_mov_b32 s94, 0\n" \
    "s_mov_b32 s95, 0\n" \
    "v_readfirstlane_b32 s53, v50\n" \
    "v_mov_b32 v58, %[ldw]\n" \
    "v_mov_b32 v59, %[ldr]\n" \
    "v_add_u32 v57, 8192, %[l16]\n" \
    "v_mov_b32 v96, %[ninf]\n" \
    "v_mov_b32 v97, %[ninf]\n" \
    "v_mov_b32 v98, %[ninf]\n" \
    "v_mov_b32 v99, %[ninf]\n" \
    "v_mov_b32 v140, -1\n" \
    "v_mov_b32 v141, -1\n" \
    "v_mov_b32 v142, -1\n" \
    "v_mov_b32 v143, -1\n" \
    "s_mov_b32 s54, 0x3e0293ee\n" \
    "s_mov_b32 s55, 0x3e0293ee\n" \
    "s_mov_b32 s52, %[kx]\n" \
    "s_cmp_eq_u32 %[w], 3\n" \
    "s_cselect_b32 s48, 0x80000, 0\n" \
    "s_add_i32 s52, s52, s48\n" \
    "s_cmp_eq_u32 %[w], 2\n" \
    "s_cselect_b32 s52, %[km], s52\n" \
    "s_cmp_eq_u32 %[w], 0\n" \
    "s_cbranch_scc0 qk_loop%=\n" \
    "v_add_u32 v57, 8192, %[kxo]\n" \
    "s_mov_b32 s49, 1\n" \
    "s_cmp_ge_u32 s53, s49\n" \
    "s_cbranch_scc1 qkp0_fk%=\n" \
    "s_mov_b32 s71, 0\n" \
    "qkp0_f%=:\n" \
    "buffer_load_dword v48, %[fk], %[rr], 0 offen sc1\n" \
    "s_waitcnt vmcnt(0)\n" \
    "v_readfirstlane_b32 s53, v48\n" \
    "v_cmp_gt_u32 vcc, s49, v48\n" \
    "s_cbranch_vccz qkp0_fk%=\n" \
    "s_sleep 2\n" \
    "s_add_i32 s71, s71, 1\n" \
    "s_cmp_lt_u32 s71, 0x40000\n" \
    "s_cbranch_scc1 qkp0_f%=\n" \
    "s_branch qk_fail%=\n" \
    "qkp0_fk%=:\n" \
    "s_lshr_b32 s75, s52, 21\n" \
    "s_xor_b32 s75, s75, s88\n" \
    "s_and_b32 s75, s75, 1\n" \
    "s_lshl_b32 s75, s75, 13\n" \
    "s_add_i32 s75, s75, s52\n" \
    "buffer_load_dwordx4 v[64:67], %[kxo], %[rr], s75 offen offset:0 sc1\n" \
    "buffer_load_dwordx4 v[68:71], %[kxo], %[rr], s75 offen offset:1024 sc1\n" \
    "buffer_load_dwordx4 v[72:75], %[kxo], %[rr], s75 offen offset:2048 sc1\n" \
    "buffer_load_dwordx4 v[76:79], %[kxo], %[rr], s75 offen offset:3072 sc1\n" \
    "buffer_load_dwordx4 v[80:83], %[kxo], %[rr], s75 offen offset:64 sc1\n" \
    "buffer_load_dwordx4 v[84:87], %[kxo], %[rr], s75 offen offset:1088 sc1\n" \
    "buffer_load_dwordx4 v[88:91], %[kxo], %[rr], s75 offen offset:2112 sc1\n" \
    "buffer_load_dwordx4 v[92:95], %[kxo], %[rr], s75 offen offset:3136 sc1\n" \
    "qk_loop%=:\n" \
    "s_mov_b32 s45, 0\n" \
    "s_cmp_eq_u32 s44, 63\n" \
    "s_cselect_b32 s89, 1, %[w]\n" \
    "qk_retry%=:\n" \
    "v_mov_b32 v32, 0\n" \
    "v_mov_b32 v33, 0\n" \
    "v_mov_b32 v34, 0\n" \
    "v_mov_b32 v35, 0\n" \
    "v_mov_b32 v36, 0\n" \
    "v_mov_b32 v37, 0\n" \
    "v_mov_b32 v38, 0\n" \
    "v_mov_b32 v39, 0\n" \
    "v_mov_b32 v40, 0\n" \
    "v_mov_b32 v41, 0\n" \
    "v_mov_b32 v42, 0\n" \
    "v_mov_b32 v43, 0\n" \
    "v_mov_b32 v44, 0\n" \
    "v_mov_b32 v45, 0\n" \
    "v_mov_b32 v46, 0\n" \
    "v_mov_b32 v47, 0\n" \
    "s_mul_i32 s72, s45, %[RT]\n" \
    "s_add_i32 s72, s72, s43\n" \
    "s_cmp_eq_u32 s44, 0\n" \
    "s_cbranch_scc1 qk_go%=\n" \
    "s_cmp_lt_u32 s45, 3\n" \
    "s_cbranch_scc1 qk_w%=\n" \
    "s_mov_b32 s71, 0\n" \
    "qka_t%=:\n" \
    "buffer_load_dwordx2 v[48:49], %[sto], %[rr], s40 offen sc1\n" \
    "s_waitcnt vmcnt(0)\n" \
    "qka_tc%=:\n" \
    "v_and_b32 v51, 0xfffffeff, v49\n" \
    "v_cmp_ne_u32 vcc, s44, v51\n" \
    "s_cbranch_vccz qka_tk%=\n" \
    "s_sleep 2\n" \
    "s_add_i32 s71, s71, 1\n" \
    "s_cmp_lt_u32 s71, 0x40000\n" \
    "s_cbranch_scc1 qka_t%=\n" \
    "s_branch qk_fail%=\n" \
    "qka_tk%=:\n" \
    "v_cmp_ne_u32 vcc, v51, v49\n" \
    "v_readfirstlane_b32 s70, v48\n" \
    "s_mov_b32 s69, vcc_lo\n" \
    "s_nop 0\n" \
    "v_subrev_u32 v48, s70, v48\n" \
    "s_nop 1\n" \
    "v_max_i32_dpp v48, v48, v48 row_ror:8 row_mask:0xf bank_mask:0xf\n" \
    "s_nop 1\n" \
    "v_max_i32_dpp v48, v48, v48 row_ror:4 row_mask:0xf bank_mask:0xf\n" \
    "s_nop 1\n" \
    "v_max_i32_dpp v48, v48, v48 row_ror:2 row_mask:0xf bank_mask:0xf\n" \
    "s_nop 1\n" \
    "v_max_i32_dpp v48, v48, v48 row_ror:1 row_mask:0xf bank_mask:0xf\n" \
    "s_nop 1\n" \
    "v_readlane_b32 s91, v48, 32\n" \
    "v_readlane_b32 s48, v48, 48\n" \
    "s_max_i32 s91, s91, s48\n" \
    "s_add_i32 s91, s91, s70\n" \
    "v_readlane_b32 s71, v48, 0\n" \
    "v_readlane_b32 s48, v48, 16\n" \
    "s_max_i32 s71, s71, s48\n" \
    "s_add_i32 s70, s70, s71\n" \
    "s_add_i32 s72, s70, %[V2]\n" \
    "qk_w%=:\n" \
    "s_memrealtime s[78:79]\n" \
    "s_getpc_b64 s[82:83]\n" \
    "qk_pc%=:\n" \
    "s_waitcnt lgkmcnt(0)\n" \
    "s_sub_i32 s48, s72, s78\n" \
    "s_add_i32 s77, s48, -4\n" \
    "s_cmp_gt_u32 s77, 2000\n" \
    "s_cbranch_scc1 qk_go%=\n" \
    "s_cmp_lt_i32 s48, 18\n" \
    "s_cbranch_scc1 qk_ns%=\n" \
    "s_sleep 2\n" \
    "s_branch qk_w%=\n" \
    "qk_ns%=:\n" \
    "s_and_b32 s48, s48, 3\n" \
    "s_sub_i32 s77, s72, s48\n" \
    "s_lshl_b32 s48, s48, 2\n" \
    "s_add_u32 s82, s82, qk_go%=-qk_pc%=\n" \
    "s_addc_u32 s83, s83, 0\n" \
    "s_sub_u32 s82, s82, s48\n" \
    "s_subb_u32 s83, s83, 0\n" \
    "s_mov_b32 s80, s78\n" \
    "qk_l%=:\n" \
    "s_memrealtime s[78:79]\n" \
    "s_waitcnt lgkmcnt(2)\n" \
    "s_sub_i32 s48, s80, s77\n" \
    "s_cmp_ge_i32 s48, 0\n" \
    "s_cbranch_scc1 qk_x%=\n" \
    "s_memrealtime s[80:81]\n" \
    "s_waitcnt lgkmcnt(2)\n" \
    "s_sub_i32 s48, s78, s77\n" \
    "s_cmp_ge_i32 s48, 0\n" \
    "s_cbranch_scc1 qk_x%=\n" \
    "s_memrealtime s[78:79]\n" \
    "s_waitcnt lgkmcnt(2)\n" \
    "s_sub_i32 s48, s80, s77\n" \
    "s_cmp_ge_i32 s48, 0\n" \
    "s_cbranch_scc1 qk_x%=\n" \
    "s_memrealtime s[80:81]\n" \
    "s_waitcnt lgkmcnt(2)\n" \
    "s_sub_i32 s48, s78, s77\n" \
    "s_cmp_ge_i32 s48, 0\n" \
    "s_cbranch_scc1 qk_x%=\n" \
    "s_memrealtime s[78:79]\n" \
    "s_waitcnt lgkmcnt(2)\n" \
    "s_sub_i32 s48, s80, s77\n" \
    "s_cmp_ge_i32 s48, 0\n" \
    "s_cbranch_scc1 qk_x%=\n" \
    "s_memrealtime s[80:81]\n" \
    "s_waitcnt lgkmcnt(2)\n" \
    "s_sub_i32 s48, s78, s77\n" \
    "s_cmp_ge_i32 s48, 0\n" \
    "s_cbranch_scc1 qk_x%=\n" \
    "s_memrealtime s[78:79]\n" \
    "s_waitcnt lgkmcnt(2)\n" \
    "s_sub_i32 s48, s80, s77\n" \
    "s_cmp_ge_i32 s48, 0\n" \
    "s_cbranch_scc1 qk_x%=\n" \
    "s_memrealtime s[80:81]\n" \
    "s_waitcnt lgkmcnt(2)\n" \
    "s_sub_i32 s48, s78, s77\n" \
    "s_cmp_ge_i32 s48, 0\n" \
    "s_cbranch_scc1 qk_x%=\n" \
    "s_branch qk_l%=\n" \
    "qk_x%=:\n" \
    "s_setpc_b64 s[82:83]\n" \
    "s_nop 5\n" \
    "s_nop 5\n" \
    "s_nop 5\n" \
    "qk_go%=:\n" \
    "buffer_load_dwordx4 v[0:3], %[l16], %[rr], s90 offen offset:0 sc1\n" \
    "s_nop 7\n" \
    "buffer_load_dwordx4 v[4:7], %[l16], %[rr], s90 offen offset:1024 sc1\n" \
    "s_nop 7\n" \
    "buffer_load_dwordx4 v[8:11], %[l16], %[rr], s90 offen offset:2048 sc1\n" \
    "s_nop 7\n" \
    "buffer_load_dwordx4 v[12:15], %[l16], %[rr], s90 offen offset:3072 sc1\n" \
    "s_nop 7\n" \
    "buffer_load_dwordx4 v[16:19], %[l16b], %[rr], s90 offen offset:0 sc1\n" \
    "s_nop 7\n" \
    "buffer_load_dwordx4 v[20:23], %[l16b], %[rr], s90 offen offset:1024 sc1\n" \
    "s_nop 7\n" \
    "buffer_load_dwordx4 v[24:27], %[l16b], %[rr], s90 offen offset:2048 sc1\n" \
    "s_nop 7\n" \
    "buffer_load_dwordx4 v[28:31], %[l16b], %[rr], s90 offen offset:3072 sc1\n" \
    "s_waitcnt vmcnt(7)\n" \
    "v_smfmac_f32_16x16x64_bf16 v[32:35], v[0:3], a[0:7], %[sidx]\n" \
    "v_smfmac_f32_16x16x64_bf16 v[36:39], v[0:3], a[8:15], %[sidx]\n" \
    "v_smfmac_f32_16x16x64_bf16 v[40:43], v[0:3], a[16:23], %[sidx]\n" \
    "v_smfmac_f32_16x16x64_bf16 v[44:47], v[0:3], a[24:31], %[sidx]\n" \
    "s_waitcnt vmcnt(6)\n" \
    "v_smfmac_f32_16x16x64_bf16 v[32:35], v[4:7], a[32:39], %[sidx]\n" \
    "v_smfmac_f32_16x16x64_bf16 v[36:39], v[4:7], a[40:47], %[sidx]\n" \
    "v_smfmac_f32_16x16x64_bf16 v[40:43], v[4:7], a[48:55], %[sidx]\n" \
    "v_smfmac_f32_16x16x64_bf16 v[44:47], v[4:7], a[56:63], %[sidx]\n" \
    "s_waitcnt vmcnt(5)\n" \
    "v_smfmac_f32_16x16x64_bf16 v[32:35], v[8:11], a[64:71], %[sidx]\n" \
    "v_smfmac_f32_16x16x64_bf16 v[36:39], v[8:11], a[72:79], %[sidx]\n" \
    "v_smfmac_f32_16x16x64_bf16 v[40:43], v[8:11], a[80:87], %[sidx]\n" \
    "v_smfmac_f32_16x16x64_bf16 v[44:47], v[8:11], a[88:95], %[sidx]\n" \
    "s_waitcnt vmcnt(4)\n" \
    "v_smfmac_f32_16x16x64_bf16 v[32:35], v[12:15], a[96:103], %[sidx]\n" \
    "v_smfmac_f32_16x16x64_bf16 v[36:39], v[12:15], a[104:111], %[sidx]\n" \
    "v_smfmac_f32_16x16x64_bf16 v[40:43], v[12:15], a[112:119], %[sidx]\n" \
    "v_smfmac_f32_16x16x64_bf16 v[44:47], v[12:15], a[120:127], %[sidx]\n" \
    "s_waitcnt vmcnt(3)\n" \
    "v_smfmac_f32_16x16x64_bf16 v[32:35], v[16:19], a[128:135], %[sidx]\n" \
    "v_smfmac_f32_16x16x64_bf16 v[36:39], v[16:19], a[136:143], %[sidx]\n" \
    "v_smfmac_f32_16x16x64_bf16 v[40:43], v[16:19], a[144:151], %[sidx]\n" \
    "v_smfmac_f32_16x16x64_bf16 v[44:47], v[16:19], a[152:159], %[sidx]\n" \
    "s_waitcnt vmcnt(2)\n" \
    "v_smfmac_f32_16x16x64_bf16 v[32:35], v[20:23], a[160:167], %[sidx]\n" \
    "v_smfmac_f32_16x16x64_bf16 v[36:39], v[20:23], a[168:175], %[sidx]\n" \
    "v_smfmac_f32_16x16x64_bf16 v[40:43], v[20:23], a[176:183], %[sidx]\n" \
    "v_smfmac_f32_16x16x64_bf16 v[44:47], v[20:23], a[184:191], %[sidx]\n" \
    "s_waitcnt vmcnt(1)\n" \
    "v_smfmac_f32_16x16x64_bf16 v[32:35], v[24:27], a[192:199], %[sidx]\n" \
    "v_smfmac_f32_16x16x64_bf16 v[36:39], v[24:27], a[200:207], %[sidx]\n" \
    "v_smfmac_f32_16x16x64_bf16 v[40:43], v[24:27], a[208:215], %[sidx]\n" \
    "v_smfmac_f32_16x16x64_bf16 v[44:47], v[24:27], a[216:223], %[sidx]\n" \
    "s_waitcnt vmcnt(0)\n" \
    "v_smfmac_f32_16x16x64_bf16 v[32:35], v[28:31], a[224:231], %[sidx]\n" \
    "v_smfmac_f32_16x16x64_bf16 v[36:39], v[28:31], a[232:239], %[sidx]\n" \
    "v_smfmac_f32_16x16x64_bf16 v[40:43], v[28:31], a[240:247], %[sidx]\n" \
    "v_smfmac_f32_16x16x64_bf16 v[44:47], v[28:31], a[248:255], %[sidx]\n" \
    "qk_md%=:\n" \
    "v_pk_add_f32 v[32:33], v[32:33], v[34:35]\n" \
    "v_pk_add_f32 v[36:37], v[36:37], v[38:39]\n" \
    "v_cvt_pk_bf16_f32 v34, v32, v33\n" \
    "v_cvt_pk_bf16_f32 v35, v36, v37\n" \
    "ds_write2_b32 v58, v34, v35 offset1:4\n" \
    "v_pk_add_f32 v[40:41], v[40:41], v[42:43]\n" \
    "v_cvt_pk_bf16_f32 v42, v40, v41\n" \
    "v_cmp_u_f32 vcc, v32, v33\n" \
    "s_cmp_lg_u32 s89, 0\n" \
    "v_pk_add_f32 v[44:45], v[44:45], v[46:47]\n" \
    "v_cvt_pk_bf16_f32 v43, v44, v45\n" \
    "ds_write2_b32 v58, v42, v43 offset0:128 offset1:132\n" \
    "ds_read_b64_tr_b16 v[100:101], v59 offset:0\n" \
    "ds_read_b64_tr_b16 v[102:103], v59 offset:512\n" \
    "s_cbranch_vccnz qk_slow%=\n" \
    "qk_ok%=:\n" \
    "s_waitcnt lgkmcnt(2)\n" \
    "s_barrier\n" \
    "s_cbranch_scc1 qk_other%=\n" \
    "ds_read_b64_tr_b16 v[104:105], v59 offset:1024\n" \
    "ds_read_b64_tr_b16 v[106:107], v59 offset:1536\n" \
    "ds_read_b64_tr_b16 v[108:109], v59 offset:2048\n" \
    "ds_read_b64_tr_b16 v[110:111], v59 offset:2560\n" \
    "ds_read_b64_tr_b16 v[112:113], v59 offset:3072\n" \
    "ds_read_b64_tr_b16 v[114:115], v59 offset:3584\n" \
    "s_waitcnt lgkmcnt(6)\n" \
    "v_mfma_f32_16x16x32_bf16 v[120:123], v[64:67], v[100:103], 0\n" \
    "buffer_load_dwordx2 v[54:55], %[sto], %[rr], s40 offen sc1\n" \
    "v_mfma_f32_16x16x32_bf16 v[124:127], v[80:83], v[100:103], 0\n" \
    "v_mfma_f32_16x16x32_bf16 v[116:119], v[100:103], v[100:103], v[96:99]\n" \
    "s_waitcnt lgkmcnt(4)\n" \
    "v_mfma_f32_16x16x32_bf16 v[120:123], v[68:71], v[104:107], v[120:123]\n" \
    "v_mfma_f32_16x16x32_bf16 v[124:127], v[84:87], v[104:107], v[124:127]\n" \
    "v_mfma_f32_16x16x32_bf16 v[116:119], v[104:107], v[104:107], v[116:119]\n" \
    "s_waitcnt lgkmcnt(2)\n" \
    "v_mfma_f32_16x16x32_bf16 v[120:123], v[72:75], v[108:111], v[120:123]\n" \
    "v_mfma_f32_16x16x32_bf16 v[124:127], v[88:91], v[108:111], v[124:127]\n" \
    "v_mfma_f32_16x16x32_bf16 v[116:119], v[108:111], v[108:111], v[116:119]\n" \
    "s_waitcnt lgkmcnt(0)\n" \
    "v_mfma_f32_16x16x32_bf16 v[120:123], v[76:79], v[112:115], v[120:123]\n" \
    "v_mfma_f32_16x16x32_bf16 v[124:127], v[92:95], v[112:115], v[124:127]\n" \
    "v_mfma_f32_16x16x32_bf16 v[116:119], v[112:115], v[112:115], v[116:119]\n" \
    "v_max3_f32 v128, v120, v121, v122\n" \
    "s_nop 2\n" \
    "v_max3_f32 v128, v128, v123, v124\n" \
    "v_max3_f32 v129, v125, v126, v127\n" \
    "v_max_f32 v130, v128, v129\n" \
    "s_nop 0\n" \
    "v_max3_f32 v131, v116, v117, v118\n" \
    "v_max3_f32 v128, v130, v131, v119\n" \
    "v_max3_f32 v129, v130, v131, v119\n" \
    "s_nop 1\n" \
    "v_permlane16_swap_b32 v128, v129\n" \
    "v_max_f32 v130, v128, v129\n" \
    "v_max_f32 v128, v128, v129\n" \
    "s_nop 1\n" \
    "v_permlane32_swap_b32 v128, v130\n" \
    "v_max_f32 v128, v128, v130\n" \
    "v_mul_f32 v128, 0xbe0293ee, v128\n" \
    "v_pk_fma_f32 v[120:121], v[120:121], s[54:55], v[128:129] op_sel_hi:[1,1,0]\n" \
    "v_pk_fma_f32 v[122:123], v[122:123], s[54:55], v[128:129] op_sel_hi:[1,1,0]\n" \
    "v_pk_fma_f32 v[124:125], v[124:125], s[54:55], v[128:129] op_sel_hi:[1,1,0]\n" \
    "v_pk_fma_f32 v[126:127], v[126:127], s[54:55], v[128:129] op_sel_hi:[1,1,0]\n" \
    "v_pk_fma_f32 v[116:117], v[116:117], s[54:55], v[128:129] op_sel_hi:[1,1,0]\n" \
    "v_pk_fma_f32 v[118:119], v[118:119], s[54:55], v[128:129] op_sel_hi:[1,1,0]\n" \
    "v_exp_f32 v120, v120\n" \
    "v_exp_f32 v121, v121\n" \
    "v_exp_f32 v122, v122\n" \
    "v_exp_f32 v123, v123\n" \
    "v_exp_f32 v124, v124\n" \
    "v_exp_f32 v125, v125\n" \
    "v_pk_add_f32 v[130:131], v[120:121], v[122:123]\n" \
    "v_exp_f32 v126, v126\n" \
    "v_exp_f32 v127, v127\n" \
    "v_exp_f32 v116, v116\n" \
    "v_exp_f32 v117, v117\n" \
    "v_pk_add_f32 v[132:133], v[124:125], v[126:127]\n" \
    "v_exp_f32 v118, v118\n" \
    "v_exp_f32 v119, v119\n" \
    "v_pk_add_f32 v[130:131], v[130:131], v[132:133]\n" \
    "v_pk_add_f32 v[132:133], v[116:117], v[118:119]\n" \
    "v_pk_add_f32 v[130:131], v[130:131], v[132:133]\n" \
    "v_add_f32 v128, v130, v131\n" \
    "v_add_f32 v129, v130, v131\n" \
    "s_nop 1\n" \
    "v_permlane16_swap_b32 v128, v129\n" \
    "v_add_f32 v130, v128, v129\n" \
    "v_add_f32 v128, v128, v129\n" \
    "s_nop 1\n" \
    "v_permlane32_swap_b32 v128, v130\n" \
    "v_add_f32 v128, v128, v130\n" \
    "v_rcp_f32 v128, v128\n" \
    "s_mov_b64 exec, %[mx]\n" \
    "v_pk_mul_f32 v[120:121], v[120:121], v[128:129] op_sel_hi:[1,0]\n" \
    "v_pk_mul_f32 v[122:123], v[122:123], v[128:129] op_sel_hi:[1,0]\n" \
    "v_pk_mul_f32 v[124:125], v[124:125], v[128:129] op_sel_hi:[1,0]\n" \
    "v_pk_mul_f32 v[126:127], v[126:127], v[128:129] op_sel_hi:[1,0]\n" \
    "v_cvt_pk_bf16_f32 v132, |v120|, |v121|\n" \
    "v_cvt_pk_bf16_f32 v133, |v122|, |v123|\n" \
    "v_cvt_pk_bf16_f32 v134, |v124|, |v125|\n" \
    "v_cvt_pk_bf16_f32 v135, |v126|, |v127|\n" \
    "buffer_store_dwordx4 v[132:135], %[pxo], %[rr], s40 offen sc1\n" \
    "buffer_store_dwordx4 v[132:135], %[pxo], %[rr], s84 offen sc1\n" \
    "v_pk_mul_f32 v[116:117], v[116:117], v[128:129] op_sel_hi:[1,0]\n" \
    "v_pk_mul_f32 v[118:119], v[118:119], v[128:129] op_sel_hi:[1,0]\n" \
    "v_cvt_pk_bf16_f32 v136, |v116|, |v117|\n" \
    "v_cvt_pk_bf16_f32 v137, |v118|, |v119|\n" \
    "s_mov_b32 exec_lo, 0\n" \
    "buffer_store_dwordx2 v[136:137], %[pmo], %[rr], s40 offen sc1\n" \
    "buffer_store_dwordx2 v[136:137], %[pmo], %[rr], s84 offen sc1\n" \
    "s_memrealtime s[46:47]\n" \
    "s_mov_b32 exec_hi, -1\n" \
    "buffer_store_dwordx2 v[140:141], %[pmo], %[rr], s42 offen sc1\n" \
    "s_mov_b64 exec, -1\n" \
    "buffer_store_dwordx4 v[140:143], %[pxo], %[rr], s42 offen sc1\n" \
    "s_add_i32 s48, s44, 1\n" \
    "s_add_i32 s49, s41, %[psw]\n" \
    "s_waitcnt lgkmcnt(0)\n" \
    "v_mov_b32 v138, s46\n" \
    "v_mov_b32 v139, s48\n" \
    "s_mov_b64 exec, 1\n" \
    "buffer_store_dwordx2 v[138:139], off, %[rr], s49 sc1\n" \
    "s_mov_b64 exec, -1\n" \
    "s_waitcnt vmcnt(7)\n" \
    "s_cmp_eq_u32 s44, 0\n" \
    "s_cbranch_scc1 qk_k1%=\n" \
    "ds_read_b128 v[64:67], v57 offset:0\n" \
    "ds_read_b128 v[68:71], v57 offset:1024\n" \
    "ds_read_b128 v[72:75], v57 offset:2048\n" \
    "ds_read_b128 v[76:79], v57 offset:3072\n" \
    "ds_read_b128 v[80:83], v57 offset:64\n" \
    "ds_read_b128 v[84:87], v57 offset:1088\n" \
    "ds_read_b128 v[88:91], v57 offset:2112\n" \
    "ds_read_b128 v[92:95], v57 offset:3136\n" \
    "v_xor_b32 v57, 0x6000, v57\n" \
    "s_branch qk_next%=\n" \
    "qk_k1%=:\n" \
    "s_mov_b32 s49, 2\n" \
    "s_cmp_ge_u32 s53, s49\n" \
    "s_cbranch_scc1 qkp1_fk%=\n" \
    "s_mov_b32 s71, 0\n" \
    "qkp1_f%=:\n" \
    "buffer_load_dword v48, %[fk], %[rr], 0 offen sc1\n" \
    "s_waitcnt vmcnt(0)\n" \
    "v_readfirstlane_b32 s53, v48\n" \
    "v_cmp_gt_u32 vcc, s49, v48\n" \
    "s_cbranch_vccz qkp1_fk%=\n" \
    "s_sleep 2\n" \
    "s_add_i32 s71, s71, 1\n" \
    "s_cmp_lt_u32 s71, 0x40000\n" \
    "s_cbranch_scc1 qkp1_f%=\n" \
    "s_branch qk_fail%=\n" \
    "qkp1_fk%=:\n" \
    "s_add_i32 s52, s52, 0x80000\n" \
    "s_lshr_b32 s75, s52, 21\n" \
    "s_xor_b32 s75, s75, s88\n" \
    "s_and_b32 s75, s75, 1\n" \
    "s_lshl_b32 s75, s75, 13\n" \
    "s_add_i32 s75, s75, s52\n" \
    "buffer_load_dwordx4 v[64:67], %[kxo], %[rr], s75 offen offset:0 sc1\n" \
    "buffer_load_dwordx4 v[68:71], %[kxo], %[rr], s75 offen offset:1024 sc1\n" \
    "buffer_load_dwordx4 v[72:75], %[kxo], %[rr], s75 offen offset:2048 sc1\n" \
    "buffer_load_dwordx4 v[76:79], %[kxo], %[rr], s75 offen offset:3072 sc1\n" \
    "buffer_load_dwordx4 v[80:83], %[kxo], %[rr], s75 offen offset:64 sc1\n" \
    "buffer_load_dwordx4 v[84:87], %[kxo], %[rr], s75 offen offset:1088 sc1\n" \
    "buffer_load_dwordx4 v[88:91], %[kxo], %[rr], s75 offen offset:2112 sc1\n" \
    "buffer_load_dwordx4 v[92:95], %[kxo], %[rr], s75 offen offset:3136 sc1\n" \
    "s_branch qk_next%=\n" \
    "qk_other%=:\n" \
    "s_cmp_eq_u32 s44, 63\n" \
    "s_cbranch_scc0 qk_nl%=\n" \
    "s_cmp_eq_u32 %[w], 2\n" \
    "s_cbranch_scc0 qk_end%=\n" \
    "v_mov_b32 v48, 64\n" \
    "s_mov_b64 exec, 1\n" \
    "buffer_store_dword v48, off, %[rr], %[fl] sc1\n" \
    "buffer_store_dword v48, %[fkm], %[rr], 0 offen sc1\n" \
    "s_mov_b64 exec, -1\n" \
    "s_branch qk_end%=\n" \
    "qk_nl%=:\n" \
    "buffer_load_dwordx2 v[54:55], %[sto], %[rr], s40 offen sc1\n" \
    "s_cmp_eq_u32 %[w], 2\n" \
    "s_cbranch_scc0 qk_o3%=\n" \
    "ds_read_b64_tr_b16 v[100:101], v59 offset:0\n" \
    "ds_read_b64_tr_b16 v[102:103], v59 offset:512\n" \
    "ds_read_b64_tr_b16 v[104:105], v59 offset:1024\n" \
    "ds_read_b64_tr_b16 v[106:107], v59 offset:1536\n" \
    "ds_read_b64_tr_b16 v[108:109], v59 offset:2048\n" \
    "ds_read_b64_tr_b16 v[110:111], v59 offset:2560\n" \
    "ds_read_b64_tr_b16 v[112:113], v59 offset:3072\n" \
    "ds_read_b64_tr_b16 v[114:115], v59 offset:3584\n" \
    "s_lshr_b32 s75, s52, 21\n" \
    "s_xor_b32 s75, s75, s88\n" \
    "s_and_b32 s75, s75, 1\n" \
    "s_lshl_b32 s75, s75, 13\n" \
    "s_add_i32 s75, s75, s52\n" \
    "s_waitcnt lgkmcnt(0)\n" \
    "s_not_b64 exec, %[mx]\n" \
    "buffer_store_dwordx4 v[100:103], %[kmo], %[rr], s75 offen offset:0 sc1\n" \
    "buffer_store_dwordx4 v[104:107], %[kmo], %[rr], s75 offen offset:512 sc1\n" \
    "buffer_store_dwordx4 v[108:111], %[kmo], %[rr], s75 offen offset:1024 sc1\n" \
    "buffer_store_dwordx4 v[112:115], %[kmo], %[rr], s75 offen offset:1536 sc1\n" \
    "s_add_i32 s52, s52, 0x20000\n" \
    "s_add_i32 s48, s44, 1\n" \
    "s_mov_b64 exec, 1\n" \
    "v_mov_b32 v48, s48\n" \
    "s_waitcnt vmcnt(0)\n" \
    "buffer_store_dword v48, %[fkm], %[rr], 0 offen sc1\n" \
    "s_mov_b64 exec, -1\n" \
    "s_branch qk_next%=\n" \
    "qk_o3%=:\n" \
    "s_cmp_eq_u32 %[w], 3\n" \
    "s_cbranch_scc1 qk_o4%=\n" \
    "s_sub_i32 s49, s44, 49\n" \
    "s_cmp_lt_u32 s49, 12\n" \
    "s_cbranch_scc0 qk_h1%=\n" \
    "s_lshl_b32 s49, s49, 10\n" \
    "v_add_u32 v68, s49, %[l16]\n" \
    "ds_write_b128 v68, v[64:67] offset:0xA000\n" \
    "qk_h1%=:\n" \
    "v_readfirstlane_b32 s53, v69\n" \
    "s_waitcnt vmcnt(0) lgkmcnt(0)\n" \
    "s_cmp_lt_u32 s44, 48\n" \
    "s_cbranch_scc0 qk_h2%=\n" \
    "buffer_load_dword v69, %[fho], %[rr], 0 offen sc1\n" \
    "s_branch qk_next%=\n" \
    "qk_h2%=:\n" \
    "s_cmp_lt_u32 s44, 60\n" \
    "s_cbranch_scc0 qk_next%=\n" \
    "s_mov_b32 s49, 2\n" \
    "s_cmp_ge_u32 s53, s49\n" \
    "s_cbranch_scc1 qkh_fk%=\n" \
    "s_mov_b32 s71, 0\n" \
    "qkh_f%=:\n" \
    "buffer_load_dword v48, %[fho], %[rr], 0 offen sc1\n" \
    "s_waitcnt vmcnt(0)\n" \
    "v_readfirstlane_b32 s53, v48\n" \
    "v_cmp_gt_u32 vcc, s49, v48\n" \
    "s_cbranch_vccz qkh_fk%=\n" \
    "s_sleep 2\n" \
    "s_add_i32 s71, s71, 1\n" \
    "s_cmp_lt_u32 s71, 0x40000\n" \
    "s_cbranch_scc1 qkh_f%=\n" \
    "s_branch qk_fail%=\n" \
    "qkh_fk%=:\n" \
    "s_sub_i32 s49, s44, 48\n" \
    "s_lshl_b32 s49, s49, 10\n" \
    "s_add_i32 s49, s49, %[ho]\n" \
    "buffer_load_dwordx4 v[64:67], %[l16], %[rr], s49 offen sc1\n" \
    "s_branch qk_next%=\n" \
    "qk_o4%=:\n" \
    "s_cmp_eq_u32 s44, 0\n" \
    "s_cbranch_scc0 qk_k3%=\n" \
    "s_mov_b32 s49, 3\n" \
    "s_cmp_ge_u32 s53, s49\n" \
    "s_cbranch_scc1 qkp3_fk%=\n" \
    "s_mov_b32 s71, 0\n" \
    "qkp3_f%=:\n" \
    "buffer_load_dword v48, %[fk], %[rr], 0 offen sc1\n" \
    "s_waitcnt vmcnt(0)\n" \
    "v_readfirstlane_b32 s53, v48\n" \
    "v_cmp_gt_u32 vcc, s49, v48\n" \
    "s_cbranch_vccz qkp3_fk%=\n" \
    "s_sleep 2\n" \
    "s_add_i32 s71, s71, 1\n" \
    "s_cmp_lt_u32 s71, 0x40000\n" \
    "s_cbranch_scc1 qkp3_f%=\n" \
    "s_branch qk_fail%=\n" \
    "qkp3_fk%=:\n" \
    "s_add_i32 s52, s52, 0x80000\n" \
    "s_lshr_b32 s75, s52, 21\n" \
    "s_xor_b32 s75, s75, s88\n" \
    "s_and_b32 s75, s75, 1\n" \
    "s_lshl_b32 s75, s75, 13\n" \
    "s_add_i32 s75, s75, s52\n" \
    "buffer_load_dwordx4 v[64:67], %[l16], %[rr], s75 offen offset:0 sc1\n" \
    "buffer_load_dwordx4 v[68:71], %[l16], %[rr], s75 offen offset:1024 sc1\n" \
    "buffer_load_dwordx4 v[72:75], %[l16], %[rr], s75 offen offset:2048 sc1\n" \
    "buffer_load_dwordx4 v[76:79], %[l16], %[rr], s75 offen offset:3072 sc1\n" \
    "buffer_load_dwordx4 v[80:83], %[l16b], %[rr], s75 offen offset:0 sc1\n" \
    "buffer_load_dwordx4 v[84:87], %[l16b], %[rr], s75 offen offset:1024 sc1\n" \
    "buffer_load_dwordx4 v[88:91], %[l16b], %[rr], s75 offen offset:2048 sc1\n" \
    "buffer_load_dwordx4 v[92:95], %[l16b], %[rr], s75 offen offset:3072 sc1\n" \
    "v_mov_b32 v56, 0\n" \
    "s_waitcnt vmcnt(0)\n" \
    "qk_k3%=:\n" \
    "ds_write_b128 v57, v[64:67] offset:0\n" \
    "ds_write_b128 v57, v[68:71] offset:1024\n" \
    "ds_write_b128 v57, v[72:75] offset:2048\n" \
    "ds_write_b128 v57, v[76:79] offset:3072\n" \
    "ds_write_b128 v57, v[80:83] offset:4096\n" \
    "ds_write_b128 v57, v[84:87] offset:5120\n" \
    "ds_write_b128 v57, v[88:91] offset:6144\n" \
    "ds_write_b128 v57, v[92:95] offset:7168\n" \
    "v_xor_b32 v57, 0x6000, v57\n" \
    "v_readfirstlane_b32 s48, v56\n" \
    "s_max_u32 s53, s53, s48\n" \
    "s_waitcnt vmcnt(0)\n" \
    "s_cmp_gt_u32 s44, 59\n" \
    "s_cbranch_scc1 qk_next%=\n" \
    "s_add_i32 s49, s44, 4\n" \
    "s_cmp_ge_u32 s53, s49\n" \
    "s_cbranch_scc1 qk3_fk%=\n" \
    "s_mov_b32 s71, 0\n" \
    "qk3_f%=:\n" \
    "buffer_load_dword v48, %[fk], %[rr], 0 offen sc1\n" \
    "s_waitcnt vmcnt(0)\n" \
    "v_readfirstlane_b32 s53, v48\n" \
    "v_cmp_gt_u32 vcc, s49, v48\n" \
    "s_cbranch_vccz qk3_fk%=\n" \
    "s_sleep 2\n" \
    "s_add_i32 s71, s71, 1\n" \
    "s_cmp_lt_u32 s71, 0x40000\n" \
    "s_cbranch_scc1 qk3_f%=\n" \
    "s_branch qk_fail%=\n" \
    "qk3_fk%=:\n" \
    "s_add_i32 s52, s52, 0x80000\n" \
    "s_waitcnt lgkmcnt(0)\n" \
    "s_lshr_b32 s75, s52, 21\n" \
    "s_xor_b32 s75, s75, s88\n" \
    "s_and_b32 s75, s75, 1\n" \
    "s_lshl_b32 s75, s75, 13\n" \
    "s_add_i32 s75, s75, s52\n" \
    "buffer_load_dwordx4 v[64:67], %[l16], %[rr], s75 offen offset:0 sc1\n" \
    "buffer_load_dwordx4 v[68:71], %[l16], %[rr], s75 offen offset:1024 sc1\n" \
    "buffer_load_dwordx4 v[72:75], %[l16], %[rr], s75 offen offset:2048 sc1\n" \
    "buffer_load_dwordx4 v[76:79], %[l16], %[rr], s75 offen offset:3072 sc1\n" \
    "buffer_load_dwordx4 v[80:83], %[l16b], %[rr], s75 offen offset:0 sc1\n" \
    "buffer_load_dwordx4 v[84:87], %[l16b], %[rr], s75 offen offset:1024 sc1\n" \
    "buffer_load_dwordx4 v[88:91], %[l16b], %[rr], s75 offen offset:2048 sc1\n" \
    "buffer_load_dwordx4 v[92:95], %[l16b], %[rr], s75 offen offset:3072 sc1\n" \
    "s_add_i32 s49, s44, 8\n" \
    "s_min_u32 s49, s49, 63\n" \
    "s_cmp_gt_u32 s53, s49\n" \
    "s_cbranch_scc1 qk_next%=\n" \
    "buffer_load_dword v56, %[fk], %[rr], 0 offen sc1\n" \
    "qk_next%=:\n" \
    "s_cmp_eq_u32 s44, 0\n" \
    "s_cbranch_scc0 qk_n1%=\n" \
    "s_mov_b32 s71, 0\n" \
    "qk_g1%=:\n" \
    "buffer_load_dword v48, off, %[rr], %[g1] sc1\n" \
    "s_waitcnt vmcnt(0)\n" \
    "v_readfirstlane_b32 s43, v48\n" \
    "s_xor_b32 s96, s43, %[odd]\n" \
    "s_and_b32 s96, s96, 2\n" \
    "s_lshl_b32 s96, s96, 13\n" \
    "s_cmp_lg_u32 s43, 0\n" \
    "s_cbranch_scc1 qk_n0%=\n" \
    "s_sleep 3\n" \
    "s_add_i32 s71, s71, 1\n" \
    "s_cmp_lt_u32 s71, 0x40000\n" \
    "s_cbranch_scc1 qk_g1%=\n" \
    "s_branch qk_fail%=\n" \
    "qk_n1%=:\n" \
    "s_mov_b32 s71, 0\n" \
    "v_mov_b32 v48, v54\n" \
    "v_mov_b32 v49, v55\n" \
    "s_branch qkb_tc%=\n" \
    "qkb_t%=:\n" \
    "buffer_load_dwordx2 v[48:49], %[sto], %[rr], s40 offen sc1\n" \
    "s_waitcnt vmcnt(0)\n" \
    "qkb_tc%=:\n" \
    "v_and_b32 v51, 0xfffffeff, v49\n" \
    "v_cmp_ne_u32 vcc, s44, v51\n" \
    "s_cbranch_vccz qkb_tk%=\n" \
    "s_sleep 2\n" \
    "s_add_i32 s71, s71, 1\n" \
    "s_cmp_lt_u32 s71, 0x40000\n" \
    "s_cbranch_scc1 qkb_t%=\n" \
    "s_branch qk_fail%=\n" \
    "qkb_tk%=:\n" \
    "v_cmp_ne_u32 vcc, v51, v49\n" \
    "v_readfirstlane_b32 s70, v48\n" \
    "s_mov_b32 s69, vcc_lo\n" \
    "s_nop 0\n" \
    "v_subrev_u32 v48, s70, v48\n" \
    "s_nop 1\n" \
    "v_max_i32_dpp v48, v48, v48 row_ror:8 row_mask:0xf bank_mask:0xf\n" \
    "s_nop 1\n" \
    "v_max_i32_dpp v48, v48, v48 row_ror:4 row_mask:0xf bank_mask:0xf\n" \
    "s_nop 1\n" \
    "v_max_i32_dpp v48, v48, v48 row_ror:2 row_mask:0xf bank_mask:0xf\n" \
    "s_nop 1\n" \
    "v_max_i32_dpp v48, v48, v48 row_ror:1 row_mask:0xf bank_mask:0xf\n" \
    "s_nop 1\n" \
    "v_readlane_b32 s91, v48, 32\n" \
    "v_readlane_b32 s48, v48, 48\n" \
    "s_max_i32 s91, s91, s48\n" \
    "s_add_i32 s91, s91, s70\n" \
    "v_readlane_b32 s71, v48, 0\n" \
    "v_readlane_b32 s48, v48, 16\n" \
    "s_max_i32 s71, s71, s48\n" \
    "s_add_i32 s70, s70, s71\n" \
    "s_sub_i32 s71, s91, s94\n" \
    "s_lshl_b32 s71, s71, 4\n" \
    "s_sub_i32 s71, s71, s92\n" \
    "s_add_i32 s73, s71, 160\n" \
    "s_cmp_lt_u32 s73, 320\n" \
    "s_cselect_b32 s71, s71, 0\n" \
    "s_ashr_i32 s71, s71, 2\n" \
    "s_add_i32 s92, s92, s71\n" \
    "s_sub_i32 s71, s70, s95\n" \
    "s_lshl_b32 s71, s71, 4\n" \
    "s_sub_i32 s71, s71, s93\n" \
    "s_add_i32 s73, s71, 160\n" \
    "s_cmp_lt_u32 s73, 288\n" \
    "s_cselect_b32 s71, s71, 0\n" \
    "s_ashr_i32 s71, s71, 2\n" \
    "s_add_i32 s93, s93, s71\n" \
    "s_add_i32 s48, s70, %[V]\n" \
    "s_sub_i32 s48, s48, s43\n" \
    "s_add_i32 s70, s70, %[V2]\n" \
    "s_cmp_eq_u32 s44, 1\n" \
    "s_cselect_b32 s69, 0, s69\n" \
    "s_add_i32 s49, s48, 16\n" \
    "s_lshl_b32 s71, %[RT], 1\n" \
    "s_add_i32 s73, s43, s71\n" \
    "s_cmp_le_i32 s49, s71\n" \
    "s_cselect_b32 s70, s73, s70\n" \
    "s_add_i32 s73, s43, %[RT]\n" \
    "s_cmp_le_i32 s49, %[RT]\n" \
    "s_cselect_b32 s70, s73, s70\n" \
    "s_add_i32 s70, s70, %[XM]\n" \
    "s_add_i32 s73, s43, 22\n" \
    "s_cmp_le_i32 s48, 0\n" \
    "s_cselect_b32 s70, s73, s70\n" \
    "s_cmp_le_i32 s48, 0\n" \
    "s_cselect_b32 s70, s43, s70\n" \
    "s_cmp_lg_u32 s69, 0\n" \
    "s_cselect_b32 s71, 3, 0\n" \
    "s_add_i32 s86, s86, s71\n" \
    "s_min_i32 s86, s86, 24\n" \
    "s_cmp_lg_u32 s69, 0\n" \
    "s_cselect_b32 s71, 8, -4\n" \
    "s_add_i32 s68, s68, s71\n" \
    "s_max_i32 s68, s68, s86\n" \
    "s_cmp_gt_i32 s48, 0\n" \
    "s_cselect_b32 s71, 3, 0\n" \
    "s_cmp_gt_i32 s48, 8\n" \
    "s_cselect_b32 s71, 0, s71\n" \
    "s_add_i32 s87, s87, s71\n" \
    "s_min_i32 s87, s87, 24\n" \
    "s_cmp_gt_i32 s48, 0\n" \
    "s_cselect_b32 s71, 6, -4\n" \
    "s_cmp_gt_i32 s48, 32\n" \
    "s_cselect_b32 s71, 0, s71\n" \
    "s_cmp_lg_u32 s69, 0\n" \
    "s_cselect_b32 s71, 3, s71\n" \
    "s_add_i32 s85, s85, s71\n" \
    "s_max_i32 s85, s85, s87\n" \
    "s_add_i32 s94, s70, 0x10000\n" \
    "s_cmp_le_i32 s48, 0\n" \
    "s_cselect_b32 s94, s70, s94\n" \
    "s_lshl_b32 s71, s68, 2\n" \
    "s_add_i32 s71, s71, s92\n" \
    "s_add_i32 s71, s71, %[A]\n" \
    "s_lshr_b32 s71, s71, 4\n" \
    "s_add_i32 s95, s70, s71\n" \
    "s_lshl_b32 s71, s44, 3\n" \
    "s_min_u32 s71, s71, 31\n" \
    "s_lshr_b32 s71, %[e1], s71\n" \
    "s_and_b32 s71, s71, 0xff\n" \
    "s_add_i32 s95, s95, s71\n" \
    "s_lshl_b32 s71, s85, 2\n" \
    "s_add_i32 s71, s71, s93\n" \
    "s_add_i32 s71, s71, %[per]\n" \
    "s_lshr_b32 s71, s71, 4\n" \
    "s_add_i32 s43, s95, s71\n" \
    "qk_n0%=:\n" \
    "v_xor_b32 v58, 4096, v58\n" \
    "v_xor_b32 v59, 4096, v59\n" \
    "s_mov_b32 s48, s40\n" \
    "s_mov_b32 s40, s41\n" \
    "s_mov_b32 s41, s42\n" \
    "s_mov_b32 s42, s48\n" \
    "s_add_i32 s44, s44, 1\n" \
    "s_add_i32 s90, s40, s96\n" \
    "s_add_i32 s84, s40, 0x4000\n" \
    "s_cmp_lt_u32 s44, 64\n" \
    "s_cbranch_scc1 qk_loop%=\n" \
    "s_branch qk_end%=\n" \
    "qk_slow%=:\n" \
    "s_add_i32 s45, s45, 1\n" \
    "s_cmp_lt_u32 s45, 3\n" \
    "s_cbranch_scc1 qk_retry%=\n" \
    "s_nop 7\n" \
    "v_max_u32 v60, v0, v1\n" \
    "v_max3_u32 v60, v60, v2, v3\n" \
    "v_max3_u32 v60, v60, v4, v5\n" \
    "v_max3_u32 v60, v60, v6, v7\n" \
    "v_max3_u32 v60, v60, v8, v9\n" \
    "v_max3_u32 v60, v60, v10, v11\n" \
    "v_max3_u32 v60, v60, v12, v13\n" \
    "v_max3_u32 v60, v60, v14, v15\n" \
    "v_max3_u32 v60, v60, v16, v17\n" \
    "v_max3_u32 v60, v60, v18, v19\n" \
    "v_max3_u32 v60, v60, v20, v21\n" \
    "v_max3_u32 v60, v60, v22, v23\n" \
    "v_max3_u32 v60, v60, v24, v25\n" \
    "v_max3_u32 v60, v60, v26, v27\n" \
    "v_max3_u32 v60, v60, v28, v29\n" \
    "v_max3_u32 v60, v60, v30, v31\n" \
    "v_cmp_eq_u32 vcc, -1, v60\n" \
    "s_cbranch_vccz qk_acc%=\n" \
    "s_add_i32 s45, s45, 1\n" \
    "s_cmp_lt_u32 s45, 0x40000\n" \
    "s_cbranch_scc1 qk_retry%=\n" \
    "s_branch qk_fail%=\n" \
    "qk_acc%=:\n" \
    "s_cmp_lg_u32 s89, 0\n" \
    "s_branch qk_ok%=\n" \
    "qk_fail%=:\n" \
    "s_mov_b32 s56, 1\n" \
    "qk_end%=:\n" \
    "s_mov_b32 m0, s76\n" \
    "s_waitcnt vmcnt(0) lgkmcnt(0)\n" \
    "s_mov_b32 %[fail], s56\n"
#define ASM_VO \
    "s_mov_b32 s76, m0\n" \
    "s_add_i32 m0, %[wl], 0\n" \
    "s_mov_b32 s48, 0x0\n" \
    "buffer_load_dwordx4 %[wva], %[wa], s48 offen  lds\n" \
    "s_add_i32 m0, %[wl], 1024\n" \
    "s_mov_b32 s48, 0x4000\n" \
    "buffer_load_dwordx4 %[wva], %[wa], s48 offen  lds\n" \
    "s_add_i32 m0, %[wl], 2048\n" \
    "s_mov_b32 s48, 0x40000\n" \
    "buffer_load_dwordx4 %[wva], %[wa], s48 offen  lds\n" \
    "s_add_i32 m0, %[wl], 3072\n" \
    "s_mov_b32 s48, 0x44000\n" \
    "buffer_load_dwordx4 %[wva], %[wa], s48 offen  lds\n" \
    "s_add_i32 m0, %[wl], 4096\n" \
    "s_mov_b32 s48, 0x80000\n" \
    "buffer_load_dwordx4 %[wva], %[wa], s48 offen  lds\n" \
    "s_add_i32 m0, %[wl], 5120\n" \
    "s_mov_b32 s48, 0x84000\n" \
    "buffer_load_dwordx4 %[wva], %[wa], s48 offen  lds\n" \
    "s_add_i32 m0, %[wl], 6144\n" \
    "s_mov_b32 s48, 0xc0000\n" \
    "buffer_load_dwordx4 %[wva], %[wa], s48 offen  lds\n" \
    "s_add_i32 m0, %[wl], 7168\n" \
    "s_mov_b32 s48, 0xc4000\n" \
    "buffer_load_dwordx4 %[wva], %[wa], s48 offen  lds\n" \
    "s_add_i32 m0, %[wl], 8192\n" \
    "s_mov_b32 s48, 0x100000\n" \
    "buffer_load_dwordx4 %[wva], %[wa], s48 offen  lds\n" \
    "s_add_i32 m0, %[wl], 9216\n" \
    "s_mov_b32 s48, 0x104000\n" \
    "buffer_load_dwordx4 %[wva], %[wa], s48 offen  lds\n" \
    "s_add_i32 m0, %[wl], 10240\n" \
    "s_mov_b32 s48, 0x140000\n" \
    "buffer_load_dwordx4 %[wva], %[wa], s48 offen  lds\n" \
    "s_add_i32 m0, %[wl], 11264\n" \
    "s_mov_b32 s48, 0x144000\n" \
    "buffer_load_dwordx4 %[wva], %[wa], s48 offen  lds\n" \
    "s_add_i32 m0, %[wl], 12288\n" \
    "s_mov_b32 s48, 0x180000\n" \
    "buffer_load_dwordx4 %[wva], %[wa], s48 offen  lds\n" \
    "s_add_i32 m0, %[wl], 13312\n" \
    "s_mov_b32 s48, 0x184000\n" \
    "buffer_load_dwordx4 %[wva], %[wa], s48 offen  lds\n" \
    "s_add_i32 m0, %[wl], 14336\n" \
    "s_mov_b32 s48, 0x1c0000\n" \
    "buffer_load_dwordx4 %[wva], %[wa], s48 offen  lds\n" \
    "s_add_i32 m0, %[wl], 15360\n" \
    "s_mov_b32 s48, 0x1c4000\n" \
    "buffer_load_dwordx4 %[wva], %[wa], s48 offen  lds\n" \
    "s_add_i32 m0, %[wl], 16384\n" \
    "s_mov_b32 s48, 0x200000\n" \
    "buffer_load_dwordx4 %[wva], %[wa], s48 offen  lds\n" \
    "s_add_i32 m0, %[wl], 17408\n" \
    "s_mov_b32 s48, 0x204000\n" \
    "buffer_load_dwordx4 %[wva], %[wa], s48 offen  lds\n" \
    "s_add_i32 m0, %[wl], 18432\n" \
    "s_mov_b32 s48, 0x240000\n" \
    "buffer_load_dwordx4 %[wva], %[wa], s48 offen  lds\n" \
    "s_add_i32 m0, %[wl], 19456\n" \
    "s_mov_b32 s48, 0x244000\n" \
    "buffer_load_dwordx4 %[wva], %[wa], s48 offen  lds\n" \
    "s_add_i32 m0, %[wl], 20480\n" \
    "s_mov_b32 s48, 0x280000\n" \
    "buffer_load_dwordx4 %[wva], %[wa], s48 offen  lds\n" \
    "s_add_i32 m0, %[wl], 21504\n" \
    "s_mov_b32 s48, 0x284000\n" \
    "buffer_load_dwordx4 %[wva], %[wa], s48 offen  lds\n" \
    "s_add_i32 m0, %[wl], 22528\n" \
    "s_mov_b32 s48, 0x2c0000\n" \
    "buffer_load_dwordx4 %[wva], %[wa], s48 offen  lds\n" \
    "s_add_i32 m0, %[wl], 23552\n" \
    "s_mov_b32 s48, 0x2c4000\n" \
    "buffer_load_dwordx4 %[wva], %[wa], s48 offen  lds\n" \
    "s_add_i32 m0, %[wl], 24576\n" \
    "s_mov_b32 s48, 0x300000\n" \
    "buffer_load_dwordx4 %[wva], %[wa], s48 offen  lds\n" \
    "s_add_i32 m0, %[wl], 25600\n" \
    "s_mov_b32 s48, 0x304000\n" \
    "buffer_load_dwordx4 %[wva], %[wa], s48 offen  lds\n" \
    "s_add_i32 m0, %[wl], 26624\n" \
    "s_mov_b32 s48, 0x340000\n" \
    "buffer_load_dwordx4 %[wva], %[wa], s48 offen  lds\n" \
    "s_add_i32 m0, %[wl], 27648\n" \
    "s_mov_b32 s48, 0x344000\n" \
    "buffer_load_dwordx4 %[wva], %[wa], s48 offen  lds\n" \
    "s_add_i32 m0, %[wl], 28672\n" \
    "s_mov_b32 s48, 0x380000\n" \
    "buffer_load_dwordx4 %[wva], %[wa], s48 offen  lds\n" \
    "s_add_i32 m0, %[wl], 29696\n" \
    "s_mov_b32 s48, 0x384000\n" \
    "buffer_load_dwordx4 %[wva], %[wa], s48 offen  lds\n" \
    "s_add_i32 m0, %[wl], 30720\n" \
    "s_mov_b32 s48, 0x3c0000\n" \
    "buffer_load_dwordx4 %[wva], %[wa], s48 offen  lds\n" \
    "s_add_i32 m0, %[wl], 31744\n" \
    "s_mov_b32 s48, 0x3c4000\n" \
    "buffer_load_dwordx4 %[wva], %[wa], s48 offen  lds\n" \
    "s_waitcnt vmcnt(28)\n" \
    "ds_read_b64_tr_b16 a[0:1], %[wr0] offset:0\n" \
    "ds_read_b64_tr_b16 a[8:9], %[wr1] offset:0\n" \
    "ds_read_b64_tr_b16 a[2:3], %[wr0] offset:1024\n" \
    "ds_read_b64_tr_b16 a[10:11], %[wr1] offset:1024\n" \
    "ds_read_b64_tr_b16 a[4:5], %[wr0] offset:2048\n" \
    "ds_read_b64_tr_b16 a[12:13], %[wr1] offset:2048\n" \
    "ds_read_b64_tr_b16 a[6:7], %[wr0] offset:3072\n" \
    "ds_read_b64_tr_b16 a[14:15], %[wr1] offset:3072\n" \
    "s_waitcnt lgkmcnt(0)\n" \
    "s_add_i32 m0, %[wl], 0\n" \
    "s_mov_b32 s48, 0x0\n" \
    "buffer_load_dwordx4 %[wvb], %[wb], s48 offen  lds\n" \
    "s_add_i32 m0, %[wl], 1024\n" \
    "s_mov_b32 s48, 0x800\n" \
    "buffer_load_dwordx4 %[wvb], %[wb], s48 offen  lds\n" \
    "s_add_i32 m0, %[wl], 2048\n" \
    "s_mov_b32 s48, 0x2000\n" \
    "buffer_load_dwordx4 %[wvb], %[wb], s48 offen  lds\n" \
    "s_add_i32 m0, %[wl], 3072\n" \
    "s_mov_b32 s48, 0x2800\n" \
    "buffer_load_dwordx4 %[wvb], %[wb], s48 offen  lds\n" \
    "s_waitcnt vmcnt(28)\n" \
    "ds_read_b64_tr_b16 a[16:17], %[wr0] offset:4096\n" \
    "ds_read_b64_tr_b16 a[24:25], %[wr1] offset:4096\n" \
    "ds_read_b64_tr_b16 a[18:19], %[wr0] offset:5120\n" \
    "ds_read_b64_tr_b16 a[26:27], %[wr1] offset:5120\n" \
    "ds_read_b64_tr_b16 a[20:21], %[wr0] offset:6144\n" \
    "ds_read_b64_tr_b16 a[28:29], %[wr1] offset:6144\n" \
    "ds_read_b64_tr_b16 a[22:23], %[wr0] offset:7168\n" \
    "ds_read_b64_tr_b16 a[30:31], %[wr1] offset:7168\n" \
    "s_waitcnt lgkmcnt(0)\n" \
    "s_add_i32 m0, %[wl], 4096\n" \
    "s_mov_b32 s48, 0x10000\n" \
    "buffer_load_dwordx4 %[wvb], %[wb], s48 offen  lds\n" \
    "s_add_i32 m0, %[wl], 5120\n" \
    "s_mov_b32 s48, 0x10800\n" \
    "buffer_load_dwordx4 %[wvb], %[wb], s48 offen  lds\n" \
    "s_add_i32 m0, %[wl], 6144\n" \
    "s_mov_b32 s48, 0x12000\n" \
    "buffer_load_dwordx4 %[wvb], %[wb], s48 offen  lds\n" \
    "s_add_i32 m0, %[wl], 7168\n" \
    "s_mov_b32 s48, 0x12800\n" \
    "buffer_load_dwordx4 %[wvb], %[wb], s48 offen  lds\n" \
    "s_waitcnt vmcnt(28)\n" \
    "ds_read_b64_tr_b16 a[32:33], %[wr0] offset:8192\n" \
    "ds_read_b64_tr_b16 a[40:41], %[wr1] offset:8192\n" \
    "ds_read_b64_tr_b16 a[34:35], %[wr0] offset:9216\n" \
    "ds_read_b64_tr_b16 a[42:43], %[wr1] offset:9216\n" \
    "ds_read_b64_tr_b16 a[36:37], %[wr0] offset:10240\n" \
    "ds_read_b64_tr_b16 a[44:45], %[wr1] offset:10240\n" \
    "ds_read_b64_tr_b16 a[38:39], %[wr0] offset:11264\n" \
    "ds_read_b64_tr_b16 a[46:47], %[wr1] offset:11264\n" \
    "s_waitcnt lgkmcnt(0)\n" \
    "s_add_i32 m0, %[wl], 8192\n" \
    "s_mov_b32 s48, 0x40\n" \
    "buffer_load_dwordx4 %[wvb], %[wb], s48 offen  lds\n" \
    "s_add_i32 m0, %[wl], 9216\n" \
    "s_mov_b32 s48, 0x840\n" \
    "buffer_load_dwordx4 %[wvb], %[wb], s48 offen  lds\n" \
    "s_add_i32 m0, %[wl], 10240\n" \
    "s_mov_b32 s48, 0x2040\n" \
    "buffer_load_dwordx4 %[wvb], %[wb], s48 offen  lds\n" \
    "s_add_i32 m0, %[wl], 11264\n" \
    "s_mov_b32 s48, 0x2840\n" \
    "buffer_load_dwordx4 %[wvb], %[wb], s48 offen  lds\n" \
    "s_waitcnt vmcnt(28)\n" \
    "ds_read_b64_tr_b16 a[48:49], %[wr0] offset:12288\n" \
    "ds_read_b64_tr_b16 a[56:57], %[wr1] offset:12288\n" \
    "ds_read_b64_tr_b16 a[50:51], %[wr0] offset:13312\n" \
    "ds_read_b64_tr_b16 a[58:59], %[wr1] offset:13312\n" \
    "ds_read_b64_tr_b16 a[52:53], %[wr0] offset:14336\n" \
    "ds_read_b64_tr_b16 a[60:61], %[wr1] offset:14336\n" \
    "ds_read_b64_tr_b16 a[54:55], %[wr0] offset:15360\n" \
    "ds_read_b64_tr_b16 a[62:63], %[wr1] offset:15360\n" \
    "s_waitcnt lgkmcnt(0)\n" \
    "s_add_i32 m0, %[wl], 12288\n" \
    "s_mov_b32 s48, 0x10040\n" \
    "buffer_load_dwordx4 %[wvb], %[wb], s48 offen  lds\n" \
    "s_add_i32 m0, %[wl], 13312\n" \
    "s_mov_b32 s48, 0x10840\n" \
    "buffer_load_dwordx4 %[wvb], %[wb], s48 offen  lds\n" \
    "s_add_i32 m0, %[wl], 14336\n" \
    "s_mov_b32 s48, 0x12040\n" \
    "buffer_load_dwordx4 %[wvb], %[wb], s48 offen  lds\n" \
    "s_add_i32 m0, %[wl], 15360\n" \
    "s_mov_b32 s48, 0x12840\n" \
    "buffer_load_dwordx4 %[wvb], %[wb], s48 offen  lds\n" \
    "s_waitcnt vmcnt(28)\n" \
    "ds_read_b64_tr_b16 a[64:65], %[wr0] offset:16384\n" \
    "ds_read_b64_tr_b16 a[72:73], %[wr1] offset:16384\n" \
    "ds_read_b64_tr_b16 a[66:67], %[wr0] offset:17408\n" \
    "ds_read_b64_tr_b16 a[74:75], %[wr1] offset:17408\n" \
    "ds_read_b64_tr_b16 a[68:69], %[wr0] offset:18432\n" \
    "ds_read_b64_tr_b16 a[76:77], %[wr1] offset:18432\n" \
    "ds_read_b64_tr_b16 a[70:71], %[wr0] offset:19456\n" \
    "ds_read_b64_tr_b16 a[78:79], %[wr1] offset:19456\n" \
    "s_waitcnt lgkmcnt(0)\n" \
    "s_add_i32 m0, %[wl], 16384\n" \
    "s_mov_b32 s48, 0x80\n" \
    "buffer_load_dwordx4 %[wvb], %[wb], s48 offen  lds\n" \
    "s_add_i32 m0, %[wl], 17408\n" \
    "s_mov_b32 s48, 0x880\n" \
    "buffer_load_dwordx4 %[wvb], %[wb], s48 offen  lds\n" \
    "s_add_i32 m0, %[wl], 18432\n" \
    "s_mov_b32 s48, 0x2080\n" \
    "buffer_load_dwordx4 %[wvb], %[wb], s48 offen  lds\n" \
    "s_add_i32 m0, %[wl], 19456\n" \
    "s_mov_b32 s48, 0x2880\n" \
    "buffer_load_dwordx4 %[wvb], %[wb], s48 offen  lds\n" \
    "s_waitcnt vmcnt(28)\n" \
    "ds_read_b64_tr_b16 a[80:81], %[wr0] offset:20480\n" \
    "ds_read_b64_tr_b16 a[88:89], %[wr1] offset:20480\n" \
    "ds_read_b64_tr_b16 a[82:83], %[wr0] offset:21504\n" \
    "ds_read_b64_tr_b16 a[90:91], %[wr1] offset:21504\n" \
    "ds_read_b64_tr_b16 a[84:85], %[wr0] offset:22528\n" \
    "ds_read_b64_tr_b16 a[92:93], %[wr1] offset:22528\n" \
    "ds_read_b64_tr_b16 a[86:87], %[wr0] offset:23552\n" \
    "ds_read_b64_tr_b16 a[94:95], %[wr1] offset:23552\n" \
    "s_waitcnt lgkmcnt(0)\n" \
    "s_add_i32 m0, %[wl], 20480\n" \
    "s_mov_b32 s48, 0x10080\n" \
    "buffer_load_dwordx4 %[wvb], %[wb], s48 offen  lds\n" \
    "s_add_i32 m0, %[wl], 21504\n" \
    "s_mov_b32 s48, 0x10880\n" \
    "buffer_load_dwordx4 %[wvb], %[wb], s48 offen  lds\n" \
    "s_add_i32 m0, %[wl], 22528\n" \
    "s_mov_b32 s48, 0x12080\n" \
    "buffer_load_dwordx4 %[wvb], %[wb], s48 offen  lds\n" \
    "s_add_i32 m0, %[wl], 23552\n" \
    "s_mov_b32 s48, 0x12880\n" \
    "buffer_load_dwordx4 %[wvb], %[wb], s48 offen  lds\n" \
    "buffer_load_dwordx2 v[48:49], off, %[rr], %[go] sc1\n" \
    "s_waitcnt vmcnt(28)\n" \
    "ds_read_b64_tr_b16 a[96:97], %[wr0] offset:24576\n" \
    "ds_read_b64_tr_b16 a[104:105], %[wr1] offset:24576\n" \
    "ds_read_b64_tr_b16 a[98:99], %[wr0] offset:25600\n" \
    "ds_read_b64_tr_b16 a[106:107], %[wr1] offset:25600\n" \
    "ds_read_b64_tr_b16 a[100:101], %[wr0] offset:26624\n" \
    "ds_read_b64_tr_b16 a[108:109], %[wr1] offset:26624\n" \
    "ds_read_b64_tr_b16 a[102:103], %[wr0] offset:27648\n" \
    "ds_read_b64_tr_b16 a[110:111], %[wr1] offset:27648\n" \
    "s_waitcnt lgkmcnt(0)\n" \
    "s_add_i32 m0, %[wl], 24576\n" \
    "s_mov_b32 s48, 0xc0\n" \
    "buffer_load_dwordx4 %[wvb], %[wb], s48 offen  lds\n" \
    "s_add_i32 m0, %[wl], 25600\n" \
    "s_mov_b32 s48, 0x8c0\n" \
    "buffer_load_dwordx4 %[wvb], %[wb], s48 offen  lds\n" \
    "s_add_i32 m0, %[wl], 26624\n" \
    "s_mov_b32 s48, 0x20c0\n" \
    "buffer_load_dwordx4 %[wvb], %[wb], s48 offen  lds\n" \
    "s_add_i32 m0, %[wl], 27648\n" \
    "s_mov_b32 s48, 0x28c0\n" \
    "buffer_load_dwordx4 %[wvb], %[wb], s48 offen  lds\n" \
    "s_waitcnt vmcnt(28)\n" \
    "ds_read_b64_tr_b16 a[112:113], %[wr0] offset:28672\n" \
    "ds_read_b64_tr_b16 a[120:121], %[wr1] offset:28672\n" \
    "ds_read_b64_tr_b16 a[114:115], %[wr0] offset:29696\n" \
    "ds_read_b64_tr_b16 a[122:123], %[wr1] offset:29696\n" \
    "ds_read_b64_tr_b16 a[116:117], %[wr0] offset:30720\n" \
    "ds_read_b64_tr_b16 a[124:125], %[wr1] offset:30720\n" \
    "ds_read_b64_tr_b16 a[118:119], %[wr0] offset:31744\n" \
    "ds_read_b64_tr_b16 a[126:127], %[wr1] offset:31744\n" \
    "s_waitcnt lgkmcnt(0)\n" \
    "s_add_i32 m0, %[wl], 28672\n" \
    "s_mov_b32 s48, 0x100c0\n" \
    "buffer_load_dwordx4 %[wvb], %[wb], s48 offen  lds\n" \
    "s_add_i32 m0, %[wl], 29696\n" \
    "s_mov_b32 s48, 0x108c0\n" \
    "buffer_load_dwordx4 %[wvb], %[wb], s48 offen  lds\n" \
    "s_add_i32 m0, %[wl], 30720\n" \
    "s_mov_b32 s48, 0x120c0\n" \
    "buffer_load_dwordx4 %[wvb], %[wb], s48 offen  lds\n" \
    "s_add_i32 m0, %[wl], 31744\n" \
    "s_mov_b32 s48, 0x128c0\n" \
    "buffer_load_dwordx4 %[wvb], %[wb], s48 offen  lds\n" \
    "s_waitcnt vmcnt(28)\n" \
    "ds_read_b64_tr_b16 a[128:129], %[wr0] offset:0\n" \
    "ds_read_b64_tr_b16 a[144:145], %[wr1] offset:0\n" \
    "ds_read_b64_tr_b16 a[130:131], %[wr0] offset:1024\n" \
    "ds_read_b64_tr_b16 a[146:147], %[wr1] offset:1024\n" \
    "ds_read_b64_tr_b16 a[132:133], %[wr0] offset:2048\n" \
    "ds_read_b64_tr_b16 a[148:149], %[wr1] offset:2048\n" \
    "ds_read_b64_tr_b16 a[134:135], %[wr0] offset:3072\n" \
    "ds_read_b64_tr_b16 a[150:151], %[wr1] offset:3072\n" \
    "s_waitcnt lgkmcnt(0)\n" \
    "s_waitcnt vmcnt(24)\n" \
    "ds_read_b64_tr_b16 a[136:137], %[wr0] offset:4096\n" \
    "ds_read_b64_tr_b16 a[152:153], %[wr1] offset:4096\n" \
    "ds_read_b64_tr_b16 a[138:139], %[wr0] offset:5120\n" \
    "ds_read_b64_tr_b16 a[154:155], %[wr1] offset:5120\n" \
    "ds_read_b64_tr_b16 a[140:141], %[wr0] offset:6144\n" \
    "ds_read_b64_tr_b16 a[156:157], %[wr1] offset:6144\n" \
    "ds_read_b64_tr_b16 a[142:143], %[wr0] offset:7168\n" \
    "ds_read_b64_tr_b16 a[158:159], %[wr1] offset:7168\n" \
    "s_waitcnt lgkmcnt(0)\n" \
    "s_waitcnt vmcnt(20)\n" \
    "ds_read_b64_tr_b16 a[160:161], %[wr0] offset:8192\n" \
    "ds_read_b64_tr_b16 a[176:177], %[wr1] offset:8192\n" \
    "ds_read_b64_tr_b16 a[162:163], %[wr0] offset:9216\n" \
    "ds_read_b64_tr_b16 a[178:179], %[wr1] offset:9216\n" \
    "ds_read_b64_tr_b16 a[164:165], %[wr0] offset:10240\n" \
    "ds_read_b64_tr_b16 a[180:181], %[wr1] offset:10240\n" \
    "ds_read_b64_tr_b16 a[166:167], %[wr0] offset:11264\n" \
    "ds_read_b64_tr_b16 a[182:183], %[wr1] offset:11264\n" \
    "s_waitcnt lgkmcnt(0)\n" \
    "s_waitcnt vmcnt(16)\n" \
    "ds_read_b64_tr_b16 a[168:169], %[wr0] offset:12288\n" \
    "ds_read_b64_tr_b16 a[184:185], %[wr1] offset:12288\n" \
    "ds_read_b64_tr_b16 a[170:171], %[wr0] offset:13312\n" \
    "ds_read_b64_tr_b16 a[186:187], %[wr1] offset:13312\n" \
    "ds_read_b64_tr_b16 a[172:173], %[wr0] offset:14336\n" \
    "ds_read_b64_tr_b16 a[188:189], %[wr1] offset:14336\n" \
    "ds_read_b64_tr_b16 a[174:175], %[wr0] offset:15360\n" \
    "ds_read_b64_tr_b16 a[190:191], %[wr1] offset:15360\n" \
    "s_waitcnt lgkmcnt(0)\n" \
    "s_waitcnt vmcnt(12)\n" \
    "ds_read_b64_tr_b16 a[192:193], %[wr0] offset:16384\n" \
    "ds_read_b64_tr_b16 a[208:209], %[wr1] offset:16384\n" \
    "ds_read_b64_tr_b16 a[194:195], %[wr0] offset:17408\n" \
    "ds_read_b64_tr_b16 a[210:211], %[wr1] offset:17408\n" \
    "ds_read_b64_tr_b16 a[196:197], %[wr0] offset:18432\n" \
    "ds_read_b64_tr_b16 a[212:213], %[wr1] offset:18432\n" \
    "ds_read_b64_tr_b16 a[198:199], %[wr0] offset:19456\n" \
    "ds_read_b64_tr_b16 a[214:215], %[wr1] offset:19456\n" \
    "s_waitcnt lgkmcnt(0)\n" \
    "s_waitcnt vmcnt(8)\n" \
    "ds_read_b64_tr_b16 a[200:201], %[wr0] offset:20480\n" \
    "ds_read_b64_tr_b16 a[216:217], %[wr1] offset:20480\n" \
    "ds_read_b64_tr_b16 a[202:203], %[wr0] offset:21504\n" \
    "ds_read_b64_tr_b16 a[218:219], %[wr1] offset:21504\n" \
    "ds_read_b64_tr_b16 a[204:205], %[wr0] offset:22528\n" \
    "ds_read_b64_tr_b16 a[220:221], %[wr1] offset:22528\n" \
    "ds_read_b64_tr_b16 a[206:207], %[wr0] offset:23552\n" \
    "ds_read_b64_tr_b16 a[222:223], %[wr1] offset:23552\n" \
    "s_waitcnt lgkmcnt(0)\n" \
    "s_waitcnt vmcnt(4)\n" \
    "ds_read_b64_tr_b16 a[224:225], %[wr0] offset:24576\n" \
    "ds_read_b64_tr_b16 a[240:241], %[wr1] offset:24576\n" \
    "ds_read_b64_tr_b16 a[226:227], %[wr0] offset:25600\n" \
    "ds_read_b64_tr_b16 a[242:243], %[wr1] offset:25600\n" \
    "ds_read_b64_tr_b16 a[228:229], %[wr0] offset:26624\n" \
    "ds_read_b64_tr_b16 a[244:245], %[wr1] offset:26624\n" \
    "ds_read_b64_tr_b16 a[230:231], %[wr0] offset:27648\n" \
    "ds_read_b64_tr_b16 a[246:247], %[wr1] offset:27648\n" \
    "s_waitcnt lgkmcnt(0)\n" \
    "s_waitcnt vmcnt(0)\n" \
    "ds_read_b64_tr_b16 a[232:233], %[wr0] offset:28672\n" \
    "ds_read_b64_tr_b16 a[248:249], %[wr1] offset:28672\n" \
    "ds_read_b64_tr_b16 a[234:235], %[wr0] offset:29696\n" \
    "ds_read_b64_tr_b16 a[250:251], %[wr1] offset:29696\n" \
    "ds_read_b64_tr_b16 a[236:237], %[wr0] offset:30720\n" \
    "ds_read_b64_tr_b16 a[252:253], %[wr1] offset:30720\n" \
    "ds_read_b64_tr_b16 a[238:239], %[wr0] offset:31744\n" \
    "ds_read_b64_tr_b16 a[254:255], %[wr1] offset:31744\n" \
    "s_waitcnt lgkmcnt(0)\n" \
    "s_barrier\n" \
    "s_mov_b32 s71, 0\n" \
    "s_mov_b32 s56, 0\n" \
    "s_branch vo_g0%=\n" \
    "vo_gt%=:\n" \
    "v_mov_b32 v50, 0\n" \
    "buffer_load_dwordx2 v[48:49], off, %[rr], %[go] sc1\n" \
    "s_waitcnt vmcnt(0)\n" \
    "vo_g0%=:\n" \
    "v_readfirstlane_b32 s88, v48\n" \
    "v_readfirstlane_b32 s49, v49\n" \
    "s_xor_b32 s48, s88, %[tlo]\n" \
    "s_lshr_b32 s48, s48, 16\n" \
    "s_xor_b32 s49, s49, %[thi]\n" \
    "s_or_b32 s48, s48, s49\n" \
    "s_cbranch_scc0 vo_gk%=\n" \
    "s_sleep 4\n" \
    "s_add_i32 s71, s71, 1\n" \
    "s_cmp_lt_u32 s71, 0x40000\n" \
    "s_cbranch_scc1 vo_gt%=\n" \
    "s_branch vo_fail%=\n" \
    "vo_gk%=:\n" \
    "s_mov_b32 s56, 2\n" \
    "s_bitcmp1_b32 s88, 1\n" \
    "s_cbranch_scc1 vo_end%=\n" \
    "s_and_b32 s88, s88, 1\n" \
    "s_and_b32 s40, s88, 1\n" \
    "s_lshl_b32 s40, s40, 13\n" \
    "s_add_i32 s41, s40, 0x20000\n" \
    "s_add_i32 s42, s40, 0x40000\n" \
    "s_mov_b32 s96, 0\n" \
    "s_mov_b32 s90, s40\n" \
    "s_mov_b32 s43, 0\n" \
    "s_mov_b32 s70, 0\n" \
    "s_mov_b32 s44, 0\n" \
    "s_mov_b32 s56, 0\n" \
    "s_mov_b32 s53, 0\n" \
    "s_mov_b32 s74, 0\n" \
    "s_mov_b32 s68, 12\n" \
    "s_mov_b32 s85, 8\n" \
    "s_mov_b32 s86, 0\n" \
    "s_mov_b32 s87, 0\n" \
    "s_add_i32 s84, s40, 0x4000\n" \
    "s_mov_b32 s92, 1520\n" \
    "s_mov_b32 s93, 848\n" \
    "s_mov_b32 s94, 0\n" \
    "s_mov_b32 s95, 0\n" \
    "v_mov_b32 v58, %[ldw]\n" \
    "v_mov_b32 v59, %[ldr]\n" \
    "v_mov_b32 v140, -1\n" \
    "v_mov_b32 v141, -1\n" \
    "v_mov_b32 v142, -1\n" \
    "v_mov_b32 v143, -1\n" \
    "vo_loop%=:\n" \
    "s_mov_b32 s45, 0\n" \
    "s_and_b32 s60, s40, 0x60000\n" \
    "s_lshl_b32 s60, s60, 2\n" \
    "s_and_b32 s48, s40, 0x2000\n" \
    "s_add_i32 s60, s60, s48\n" \
    "s_and_b32 s61, s42, 0x60000\n" \
    "s_lshl_b32 s61, s61, 2\n" \
    "s_and_b32 s48, s42, 0x2000\n" \
    "s_add_i32 s61, s61, s48\n" \
    "s_add_i32 s74, s44, 2\n" \
    "s_min_u32 s74, s74, 63\n" \
    "s_xnor_b32 s49, s74, s88\n" \
    "s_cmp_lt_u32 s74, 4\n" \
    "s_addc_u32 s49, s49, 0\n" \
    "s_and_b32 s49, s49, 1\n" \
    "s_lshl_b32 s49, s49, 13\n" \
    "s_lshl_b32 s74, s74, 21\n" \
    "s_add_i32 s74, s74, s49\n" \
    "v_mov_b32 v64, 0\n" \
    "v_mov_b32 v65, 0\n" \
    "v_mov_b32 v66, 0\n" \
    "v_mov_b32 v67, 0\n" \
    "v_mov_b32 v68, 0\n" \
    "v_mov_b32 v69, 0\n" \
    "v_mov_b32 v70, 0\n" \
    "v_mov_b32 v71, 0\n" \
    "v_mov_b32 v72, 0\n" \
    "v_mov_b32 v73, 0\n" \
    "v_mov_b32 v74, 0\n" \
    "v_mov_b32 v75, 0\n" \
    "v_mov_b32 v76, 0\n" \
    "v_mov_b32 v77, 0\n" \
    "v_mov_b32 v78, 0\n" \
    "v_mov_b32 v79, 0\n" \
    "v_mov_b32 v80, 0\n" \
    "v_mov_b32 v81, 0\n" \
    "v_mov_b32 v82, 0\n" \
    "v_mov_b32 v83, 0\n" \
    "v_mov_b32 v84, 0\n" \
    "v_mov_b32 v85, 0\n" \
    "v_mov_b32 v86, 0\n" \
    "v_mov_b32 v87, 0\n" \
    "v_mov_b32 v88, 0\n" \
    "v_mov_b32 v89, 0\n" \
    "v_mov_b32 v90, 0\n" \
    "v_mov_b32 v91, 0\n" \
    "v_mov_b32 v92, 0\n" \
    "v_mov_b32 v93, 0\n" \
    "v_mov_b32 v94, 0\n" \
    "v_mov_b32 v95, 0\n" \
    "vo_retry%=:\n" \
    "v_mov_b32 v32, 0\n" \
    "v_mov_b32 v33, 0\n" \
    "v_mov_b32 v34, 0\n" \
    "v_mov_b32 v35, 0\n" \
    "v_mov_b32 v36, 0\n" \
    "v_mov_b32 v37, 0\n" \
    "v_mov_b32 v38, 0\n" \
    "v_mov_b32 v39, 0\n" \
    "s_mul_i32 s72, s45, %[RT]\n" \
    "s_add_i32 s72, s72, s43\n" \
    "s_cmp_eq_u32 s44, 0\n" \
    "s_cbranch_scc1 vo_go%=\n" \
    "s_cmp_lt_u32 s45, 3\n" \
    "s_cbranch_scc1 vo_w%=\n" \
    "s_mov_b32 s71, 0\n" \
    "voa_t%=:\n" \
    "buffer_load_dwordx2 v[48:49], %[sto], %[rr], s40 offen sc1\n" \
    "s_waitcnt vmcnt(0)\n" \
    "voa_tc%=:\n" \
    "v_and_b32 v51, 0xfffffeff, v49\n" \
    "v_cmp_ne_u32 vcc, s44, v51\n" \
    "s_cbranch_vccz voa_tk%=\n" \
    "s_sleep 2\n" \
    "s_add_i32 s71, s71, 1\n" \
    "s_cmp_lt_u32 s71, 0x40000\n" \
    "s_cbranch_scc1 voa_t%=\n" \
    "s_branch vo_fail%=\n" \
    "voa_tk%=:\n" \
    "v_cmp_ne_u32 vcc, v51, v49\n" \
    "v_readfirstlane_b32 s70, v48\n" \
    "s_mov_b32 s69, vcc_lo\n" \
    "s_nop 0\n" \
    "v_subrev_u32 v48, s70, v48\n" \
    "s_nop 1\n" \
    "v_max_i32_dpp v48, v48, v48 row_ror:8 row_mask:0xf bank_mask:0xf\n" \
    "s_nop 1\n" \
    "v_max_i32_dpp v48, v48, v48 row_ror:4 row_mask:0xf bank_mask:0xf\n" \
    "s_nop 1\n" \
    "v_max_i32_dpp v48, v48, v48 row_ror:2 row_mask:0xf bank_mask:0xf\n" \
    "s_nop 1\n" \
    "v_max_i32_dpp v48, v48, v48 row_ror:1 row_mask:0xf bank_mask:0xf\n" \
    "s_nop 1\n" \
    "v_readlane_b32 s91, v48, 32\n" \
    "v_readlane_b32 s48, v48, 48\n" \
    "s_max_i32 s91, s91, s48\n" \
    "s_add_i32 s91, s91, s70\n" \
    "v_readlane_b32 s71, v48, 0\n" \
    "v_readlane_b32 s48, v48, 16\n" \
    "s_max_i32 s71, s71, s48\n" \
    "s_add_i32 s70, s70, s71\n" \
    "s_add_i32 s72, s70, %[V2]\n" \
    "vo_w%=:\n" \
    "s_memrealtime s[78:79]\n" \
    "s_getpc_b64 s[82:83]\n" \
    "vo_pc%=:\n" \
    "s_waitcnt lgkmcnt(0)\n" \
    "s_sub_i32 s48, s72, s78\n" \
    "s_add_i32 s77, s48, -4\n" \
    "s_cmp_gt_u32 s77, 2000\n" \
    "s_cbranch_scc1 vo_go%=\n" \
    "s_cmp_lt_i32 s48, 18\n" \
    "s_cbranch_scc1 vo_ns%=\n" \
    "s_sleep 2\n" \
    "s_branch vo_w%=\n" \
    "vo_ns%=:\n" \
    "s_and_b32 s48, s48, 3\n" \
    "s_sub_i32 s77, s72, s48\n" \
    "s_lshl_b32 s48, s48, 2\n" \
    "s_add_u32 s82, s82, vo_go%=-vo_pc%=\n" \
    "s_addc_u32 s83, s83, 0\n" \
    "s_sub_u32 s82, s82, s48\n" \
    "s_subb_u32 s83, s83, 0\n" \
    "s_mov_b32 s80, s78\n" \
    "vo_l%=:\n" \
    "s_memrealtime s[78:79]\n" \
    "s_waitcnt lgkmcnt(2)\n" \
    "s_sub_i32 s48, s80, s77\n" \
    "s_cmp_ge_i32 s48, 0\n" \
    "s_cbranch_scc1 vo_x%=\n" \
    "s_memrealtime s[80:81]\n" \
    "s_waitcnt lgkmcnt(2)\n" \
    "s_sub_i32 s48, s78, s77\n" \
    "s_cmp_ge_i32 s48, 0\n" \
    "s_cbranch_scc1 vo_x%=\n" \
    "s_memrealtime s[78:79]\n" \
    "s_waitcnt lgkmcnt(2)\n" \
    "s_sub_i32 s48, s80, s77\n" \
    "s_cmp_ge_i32 s48, 0\n" \
    "s_cbranch_scc1 vo_x%=\n" \
    "s_memrealtime s[80:81]\n" \
    "s_waitcnt lgkmcnt(2)\n" \
    "s_sub_i32 s48, s78, s77\n" \
    "s_cmp_ge_i32 s48, 0\n" \
    "s_cbranch_scc1 vo_x%=\n" \
    "s_memrealtime s[78:79]\n" \
    "s_waitcnt lgkmcnt(2)\n" \
    "s_sub_i32 s48, s80, s77\n" \
    "s_cmp_ge_i32 s48, 0\n" \
    "s_cbranch_scc1 vo_x%=\n" \
    "s_memrealtime s[80:81]\n" \
    "s_waitcnt lgkmcnt(2)\n" \
    "s_sub_i32 s48, s78, s77\n" \
    "s_cmp_ge_i32 s48, 0\n" \
    "s_cbranch_scc1 vo_x%=\n" \
    "s_memrealtime s[78:79]\n" \
    "s_waitcnt lgkmcnt(2)\n" \
    "s_sub_i32 s48, s80, s77\n" \
    "s_cmp_ge_i32 s48, 0\n" \
    "s_cbranch_scc1 vo_x%=\n" \
    "s_memrealtime s[80:81]\n" \
    "s_waitcnt lgkmcnt(2)\n" \
    "s_sub_i32 s48, s78, s77\n" \
    "s_cmp_ge_i32 s48, 0\n" \
    "s_cbranch_scc1 vo_x%=\n" \
    "s_branch vo_l%=\n" \
    "vo_x%=:\n" \
    "s_setpc_b64 s[82:83]\n" \
    "s_nop 5\n" \
    "s_nop 5\n" \
    "s_nop 5\n" \
    "vo_go%=:\n" \
    "buffer_load_dwordx4 v[0:3], %[l16], %[rr], s90 offen offset:0 sc1\n" \
    "s_nop 7\n" \
    "buffer_load_dwordx4 v[4:7], %[l16], %[rr], s90 offen offset:1024 sc1\n" \
    "s_nop 7\n" \
    "buffer_load_dwordx4 v[8:11], %[l16], %[rr], s90 offen offset:2048 sc1\n" \
    "s_nop 7\n" \
    "buffer_load_dwordx4 v[12:15], %[l16], %[rr], s90 offen offset:3072 sc1\n" \
    "s_nop 7\n" \
    "buffer_load_dwordx4 v[16:19], %[l16b], %[rr], s90 offen offset:0 sc1\n" \
    "s_nop 7\n" \
    "buffer_load_dwordx4 v[20:23], %[l16b], %[rr], s90 offen offset:1024 sc1\n" \
    "s_nop 7\n" \
    "buffer_load_dwordx4 v[24:27], %[l16b], %[rr], s90 offen offset:2048 sc1\n" \
    "s_nop 7\n" \
    "buffer_load_dwordx4 v[28:31], %[l16b], %[rr], s90 offen offset:3072 sc1\n" \
    "s_waitcnt vmcnt(7)\n" \
    "v_smfmac_f32_16x16x64_bf16 v[32:35], v[0:3], a[0:7], %[sidx]\n" \
    "v_smfmac_f32_16x16x64_bf16 v[36:39], v[0:3], a[8:15], %[sidx]\n" \
    "s_waitcnt vmcnt(6)\n" \
    "v_smfmac_f32_16x16x64_bf16 v[32:35], v[4:7], a[16:23], %[sidx]\n" \
    "v_smfmac_f32_16x16x64_bf16 v[36:39], v[4:7], a[24:31], %[sidx]\n" \
    "s_waitcnt vmcnt(5)\n" \
    "v_smfmac_f32_16x16x64_bf16 v[32:35], v[8:11], a[32:39], %[sidx]\n" \
    "v_smfmac_f32_16x16x64_bf16 v[36:39], v[8:11], a[40:47], %[sidx]\n" \
    "s_waitcnt vmcnt(4)\n" \
    "v_smfmac_f32_16x16x64_bf16 v[32:35], v[12:15], a[48:55], %[sidx]\n" \
    "v_smfmac_f32_16x16x64_bf16 v[36:39], v[12:15], a[56:63], %[sidx]\n" \
    "s_waitcnt vmcnt(3)\n" \
    "v_smfmac_f32_16x16x64_bf16 v[32:35], v[16:19], a[64:71], %[sidx]\n" \
    "v_smfmac_f32_16x16x64_bf16 v[36:39], v[16:19], a[72:79], %[sidx]\n" \
    "s_waitcnt vmcnt(2)\n" \
    "v_smfmac_f32_16x16x64_bf16 v[32:35], v[20:23], a[80:87], %[sidx]\n" \
    "v_smfmac_f32_16x16x64_bf16 v[36:39], v[20:23], a[88:95], %[sidx]\n" \
    "s_waitcnt vmcnt(1)\n" \
    "v_smfmac_f32_16x16x64_bf16 v[32:35], v[24:27], a[96:103], %[sidx]\n" \
    "v_smfmac_f32_16x16x64_bf16 v[36:39], v[24:27], a[104:111], %[sidx]\n" \
    "s_waitcnt vmcnt(0)\n" \
    "v_smfmac_f32_16x16x64_bf16 v[32:35], v[28:31], a[112:119], %[sidx]\n" \
    "v_smfmac_f32_16x16x64_bf16 v[36:39], v[28:31], a[120:127], %[sidx]\n" \
    "vo_md%=:\n" \
    "s_nop 4\n" \
    "v_pk_add_f32 v[32:33], v[32:33], v[34:35]\n" \
    "v_cvt_pk_bf16_f32 v34, v32, v33\n" \
    "s_nop 0\n" \
    "v_pk_add_f32 v[36:37], v[36:37], v[38:39]\n" \
    "v_cvt_pk_bf16_f32 v35, v36, v37\n" \
    "ds_write_b64 v58, v[34:35]\n" \
    "v_cmp_u_f32 vcc, v32, v33\n" \
    "s_cbranch_vccnz vo_slow%=\n" \
    "vo_ok%=:\n" \
    "s_waitcnt lgkmcnt(0)\n" \
    "s_barrier\n" \
    "ds_read_b64_tr_b16 v[100:101], v59 offset:0\n" \
    "ds_read_b64_tr_b16 v[102:103], v59 offset:512\n" \
    "ds_read_b64_tr_b16 v[104:105], v59 offset:1024\n" \
    "ds_read_b64_tr_b16 v[106:107], v59 offset:1536\n" \
    "buffer_load_dwordx2 v[54:55], %[sto], %[rr], s40 offen sc1\n" \
    "buffer_load_dword v52, %[pf0], %[rr], s74 offen sc1\n" \
    "buffer_load_dword v53, %[pf1], %[rr], s74 offen sc1\n" \
    "s_waitcnt lgkmcnt(2)\n" \
    "v_smfmac_f32_16x16x64_bf16 v[64:67], v[100:103], a[128:135], %[sidx]\n" \
    "v_smfmac_f32_16x16x64_bf16 v[72:75], v[100:103], a[160:167], %[sidx]\n" \
    "s_waitcnt lgkmcnt(0)\n" \
    "v_smfmac_f32_16x16x64_bf16 v[64:67], v[104:107], a[136:143], %[sidx]\n" \
    "v_smfmac_f32_16x16x64_bf16 v[72:75], v[104:107], a[168:175], %[sidx]\n" \
    "v_smfmac_f32_16x16x64_bf16 v[68:71], v[100:103], a[144:151], %[sidx]\n" \
    "v_smfmac_f32_16x16x64_bf16 v[68:71], v[104:107], a[152:159], %[sidx]\n" \
    "v_smfmac_f32_16x16x64_bf16 v[76:79], v[100:103], a[176:183], %[sidx]\n" \
    "v_smfmac_f32_16x16x64_bf16 v[76:79], v[104:107], a[184:191], %[sidx]\n" \
    "s_nop 1\n" \
    "v_smfmac_f32_16x16x64_bf16 v[80:83], v[100:103], a[192:199], %[sidx]\n" \
    "v_pk_add_f32 v[64:65], v[64:65], v[66:67]\n" \
    "v_cvt_pk_bf16_f32 v110, v64, v65\n" \
    "v_smfmac_f32_16x16x64_bf16 v[80:83], v[104:107], a[200:207], %[sidx]\n" \
    "v_min_u32 v110, -2, v110\n" \
    "v_pk_add_f32 v[72:73], v[72:73], v[74:75]\n" \
    "v_smfmac_f32_16x16x64_bf16 v[88:91], v[100:103], a[224:231], %[sidx]\n" \
    "v_cvt_pk_bf16_f32 v111, v72, v73\n" \
    "v_min_u32 v111, -2, v111\n" \
    "v_smfmac_f32_16x16x64_bf16 v[88:91], v[104:107], a[232:239], %[sidx]\n" \
    "v_pk_add_f32 v[68:69], v[68:69], v[70:71]\n" \
    "v_cvt_pk_bf16_f32 v112, v68, v69\n" \
    "v_smfmac_f32_16x16x64_bf16 v[84:87], v[100:103], a[208:215], %[sidx]\n" \
    "v_min_u32 v112, -2, v112\n" \
    "v_pk_add_f32 v[76:77], v[76:77], v[78:79]\n" \
    "v_smfmac_f32_16x16x64_bf16 v[84:87], v[104:107], a[216:223], %[sidx]\n" \
    "v_cvt_pk_bf16_f32 v113, v76, v77\n" \
    "v_min_u32 v113, -2, v113\n" \
    "v_smfmac_f32_16x16x64_bf16 v[92:95], v[100:103], a[240:247], %[sidx]\n" \
    "v_permlane32_swap_b32 v110, v111\n" \
    "s_nop 0\n" \
    "v_smfmac_f32_16x16x64_bf16 v[92:95], v[104:107], a[248:255], %[sidx]\n" \
    "v_permlane32_swap_b32 v112, v113\n" \
    "s_nop 1\n" \
    "v_permlane16_swap_b32 v110, v112\n" \
    "v_permlane16_swap_b32 v111, v113\n" \
    "buffer_store_dwordx4 v[110:113], %[uo], %[rr], s60 offen sc1\n" \
    "v_pk_add_f32 v[80:81], v[80:81], v[82:83]\n" \
    "v_cvt_pk_bf16_f32 v114, v80, v81\n" \
    "v_min_u32 v114, -2, v114\n" \
    "v_pk_add_f32 v[88:89], v[88:89], v[90:91]\n" \
    "v_cvt_pk_bf16_f32 v115, v88, v89\n" \
    "v_min_u32 v115, -2, v115\n" \
    "v_pk_add_f32 v[84:85], v[84:85], v[86:87]\n" \
    "v_cvt_pk_bf16_f32 v116, v84, v85\n" \
    "v_min_u32 v116, -2, v116\n" \
    "v_pk_add_f32 v[92:93], v[92:93], v[94:95]\n" \
    "v_cvt_pk_bf16_f32 v117, v92, v93\n" \
    "v_min_u32 v117, -2, v117\n" \
    "v_permlane32_swap_b32 v114, v115\n" \
    "s_nop 0\n" \
    "v_permlane32_swap_b32 v116, v117\n" \
    "s_nop 1\n" \
    "v_permlane16_swap_b32 v114, v116\n" \
    "v_permlane16_swap_b32 v115, v117\n" \
    "buffer_store_dwordx4 v[114:117], %[uo2], %[rr], s60 offen sc1\n" \
    "s_waitcnt vmcnt(4)\n" \
    "s_cmp_eq_u32 s44, 63\n" \
    "s_cbranch_scc1 vo_end%=\n" \
    "s_cmp_eq_u32 s44, 0\n" \
    "s_cbranch_scc0 vo_n1%=\n" \
    "s_mov_b32 s71, 0\n" \
    "vo_g1%=:\n" \
    "buffer_load_dword v48, off, %[rr], %[g1] sc1\n" \
    "s_waitcnt vmcnt(0)\n" \
    "v_readfirstlane_b32 s43, v48\n" \
    "s_xor_b32 s96, s43, %[odd]\n" \
    "s_and_b32 s96, s96, 2\n" \
    "s_lshl_b32 s96, s96, 13\n" \
    "s_cmp_lg_u32 s43, 0\n" \
    "s_cbranch_scc1 vo_n0%=\n" \
    "s_sleep 3\n" \
    "s_add_i32 s71, s71, 1\n" \
    "s_cmp_lt_u32 s71, 0x40000\n" \
    "s_cbranch_scc1 vo_g1%=\n" \
    "s_branch vo_fail%=\n" \
    "vo_n1%=:\n" \
    "s_mov_b32 s71, 0\n" \
    "v_mov_b32 v48, v54\n" \
    "v_mov_b32 v49, v55\n" \
    "s_branch vob_tc%=\n" \
    "vob_t%=:\n" \
    "buffer_load_dwordx2 v[48:49], %[sto], %[rr], s40 offen sc1\n" \
    "s_waitcnt vmcnt(0)\n" \
    "vob_tc%=:\n" \
    "v_and_b32 v51, 0xfffffeff, v49\n" \
    "v_cmp_ne_u32 vcc, s44, v51\n" \
    "s_cbranch_vccz vob_tk%=\n" \
    "s_sleep 2\n" \
    "s_add_i32 s71, s71, 1\n" \
    "s_cmp_lt_u32 s71, 0x40000\n" \
    "s_cbranch_scc1 vob_t%=\n" \
    "s_branch vo_fail%=\n" \
    "vob_tk%=:\n" \
    "v_cmp_ne_u32 vcc, v51, v49\n" \
    "v_readfirstlane_b32 s70, v48\n" \
    "s_mov_b32 s69, vcc_lo\n" \
    "s_nop 0\n" \
    "v_subrev_u32 v48, s70, v48\n" \
    "s_nop 1\n" \
    "v_max_i32_dpp v48, v48, v48 row_ror:8 row_mask:0xf bank_mask:0xf\n" \
    "s_nop 1\n" \
    "v_max_i32_dpp v48, v48, v48 row_ror:4 row_mask:0xf bank_mask:0xf\n" \
    "s_nop 1\n" \
    "v_max_i32_dpp v48, v48, v48 row_ror:2 row_mask:0xf bank_mask:0xf\n" \
    "s_nop 1\n" \
    "v_max_i32_dpp v48, v48, v48 row_ror:1 row_mask:0xf bank_mask:0xf\n" \
    "s_nop 1\n" \
    "v_readlane_b32 s91, v48, 32\n" \
    "v_readlane_b32 s48, v48, 48\n" \
    "s_max_i32 s91, s91, s48\n" \
    "s_add_i32 s91, s91, s70\n" \
    "v_readlane_b32 s71, v48, 0\n" \
    "v_readlane_b32 s48, v48, 16\n" \
    "s_max_i32 s71, s71, s48\n" \
    "s_add_i32 s70, s70, s71\n" \
    "s_sub_i32 s71, s91, s94\n" \
    "s_lshl_b32 s71, s71, 4\n" \
    "s_sub_i32 s71, s71, s92\n" \
    "s_add_i32 s73, s71, 160\n" \
    "s_cmp_lt_u32 s73, 320\n" \
    "s_cselect_b32 s71, s71, 0\n" \
    "s_ashr_i32 s71, s71, 2\n" \
    "s_add_i32 s92, s92, s71\n" \
    "s_sub_i32 s71, s70, s95\n" \
    "s_lshl_b32 s71, s71, 4\n" \
    "s_sub_i32 s71, s71, s93\n" \
    "s_add_i32 s73, s71, 160\n" \
    "s_cmp_lt_u32 s73, 288\n" \
    "s_cselect_b32 s71, s71, 0\n" \
    "s_ashr_i32 s71, s71, 2\n" \
    "s_add_i32 s93, s93, s71\n" \
    "s_add_i32 s48, s70, %[V]\n" \
    "s_sub_i32 s48, s48, s43\n" \
    "s_add_i32 s70, s70, %[V2]\n" \
    "s_cmp_eq_u32 s44, 1\n" \
    "s_cselect_b32 s69, 0, s69\n" \
    "s_add_i32 s49, s48, 16\n" \
    "s_lshl_b32 s71, %[RT], 1\n" \
    "s_add_i32 s73, s43, s71\n" \
    "s_cmp_le_i32 s49, s71\n" \
    "s_cselect_b32 s70, s73, s70\n" \
    "s_add_i32 s73, s43, %[RT]\n" \
    "s_cmp_le_i32 s49, %[RT]\n" \
    "s_cselect_b32 s70, s73, s70\n" \
    "s_add_i32 s70, s70, %[XM]\n" \
    "s_add_i32 s73, s43, 22\n" \
    "s_cmp_le_i32 s48, 0\n" \
    "s_cselect_b32 s70, s73, s70\n" \
    "s_cmp_le_i32 s48, 0\n" \
    "s_cselect_b32 s70, s43, s70\n" \
    "s_cmp_lg_u32 s69, 0\n" \
    "s_cselect_b32 s71, 3, 0\n" \
    "s_add_i32 s86, s86, s71\n" \
    "s_min_i32 s86, s86, 24\n" \
    "s_cmp_lg_u32 s69, 0\n" \
    "s_cselect_b32 s71, 8, -4\n" \
    "s_add_i32 s68, s68, s71\n" \
    "s_max_i32 s68, s68, s86\n" \
    "s_cmp_gt_i32 s48, 0\n" \
    "s_cselect_b32 s71, 3, 0\n" \
    "s_cmp_gt_i32 s48, 8\n" \
    "s_cselect_b32 s71, 0, s71\n" \
    "s_add_i32 s87, s87, s71\n" \
    "s_min_i32 s87, s87, 24\n" \
    "s_cmp_gt_i32 s48, 0\n" \
    "s_cselect_b32 s71, 6, -4\n" \
    "s_cmp_gt_i32 s48, 32\n" \
    "s_cselect_b32 s71, 0, s71\n" \
    "s_cmp_lg_u32 s69, 0\n" \
    "s_cselect_b32 s71, 3, s71\n" \
    "s_add_i32 s85, s85, s71\n" \
    "s_max_i32 s85, s85, s87\n" \
    "s_add_i32 s94, s70, 0x10000\n" \
    "s_cmp_le_i32 s48, 0\n" \
    "s_cselect_b32 s94, s70, s94\n" \
    "s_lshl_b32 s71, s68, 2\n" \
    "s_add_i32 s71, s71, s92\n" \
    "s_add_i32 s71, s71, %[A]\n" \
    "s_lshr_b32 s71, s71, 4\n" \
    "s_add_i32 s95, s70, s71\n" \
    "s_lshl_b32 s71, s44, 3\n" \
    "s_min_u32 s71, s71, 31\n" \
    "s_lshr_b32 s71, %[e1], s71\n" \
    "s_and_b32 s71, s71, 0xff\n" \
    "s_add_i32 s95, s95, s71\n" \
    "s_lshl_b32 s71, s85, 2\n" \
    "s_add_i32 s71, s71, s93\n" \
    "s_add_i32 s71, s71, %[per]\n" \
    "s_lshr_b32 s71, s71, 4\n" \
    "s_add_i32 s43, s95, s71\n" \
    "vo_n0%=:\n" \
    "s_cmp_gt_u32 s44, 61\n" \
    "s_cbranch_scc1 vo_nz%=\n" \
    "s_cmp_eq_u32 s44, 0\n" \
    "s_cbranch_scc1 vo_pz%=\n" \
    "s_sub_i32 s72, s43, %[PZ]\n" \
    "vop_w%=:\n" \
    "s_sleep 1\n" \
    "s_memrealtime s[78:79]\n" \
    "s_waitcnt lgkmcnt(0)\n" \
    "s_sub_i32 s48, s72, s78\n" \
    "s_add_i32 s48, s48, -1\n" \
    "s_cmp_lt_u32 s48, 2000\n" \
    "s_cbranch_scc1 vop_w%=\n" \
    "vo_pz%=:\n" \
    "buffer_store_dwordx4 v[140:143], %[uo], %[rr], s61 offen sc1\n" \
    "buffer_store_dwordx4 v[140:143], %[uo2], %[rr], s61 offen sc1\n" \
    "vo_nz%=:\n" \
    "v_xor_b32 v58, 2048, v58\n" \
    "v_xor_b32 v59, 2048, v59\n" \
    "s_mov_b32 s48, s40\n" \
    "s_mov_b32 s40, s41\n" \
    "s_mov_b32 s41, s42\n" \
    "s_mov_b32 s42, s48\n" \
    "s_add_i32 s44, s44, 1\n" \
    "s_add_i32 s90, s40, s96\n" \
    "s_add_i32 s84, s40, 0x4000\n" \
    "s_branch vo_loop%=\n" \
    "vo_slow%=:\n" \
    "s_add_i32 s45, s45, 1\n" \
    "s_cmp_lt_u32 s45, 3\n" \
    "s_cbranch_scc1 vo_retry%=\n" \
    "s_nop 7\n" \
    "v_max_u32 v60, v0, v1\n" \
    "v_max3_u32 v60, v60, v2, v3\n" \
    "v_max3_u32 v60, v60, v4, v5\n" \
    "v_max3_u32 v60, v60, v6, v7\n" \
    "v_max3_u32 v60, v60, v8, v9\n" \
    "v_max3_u32 v60, v60, v10, v11\n" \
    "v_max3_u32 v60, v60, v12, v13\n" \
    "v_max3_u32 v60, v60, v14, v15\n" \
    "v_max3_u32 v60, v60, v16, v17\n" \
    "v_max3_u32 v60, v60, v18, v19\n" \
    "v_max3_u32 v60, v60, v20, v21\n" \
    "v_max3_u32 v60, v60, v22, v23\n" \
    "v_max3_u32 v60, v60, v24, v25\n" \
    "v_max3_u32 v60, v60, v26, v27\n" \
    "v_max3_u32 v60, v60, v28, v29\n" \
    "v_max3_u32 v60, v60, v30, v31\n" \
    "v_cmp_eq_u32 vcc, -1, v60\n" \
    "s_cbranch_vccz vo_ok%=\n" \
    "s_add_i32 s45, s45, 1\n" \
    "s_cmp_lt_u32 s45, 0x40000\n" \
    "s_cbranch_scc1 vo_retry%=\n" \
    "s_branch vo_fail%=\n" \
    "vo_fail%=:\n" \
    "s_mov_b32 s56, 1\n" \
    "vo_end%=:\n" \
    "s_mov_b32 m0, s76\n" \
    "s_waitcnt vmcnt(0) lgkmcnt(0)\n" \
    "s_mov_b32 %[fail], s56\n"
#define ASM_OW \
    "s_mov_b32 s88, %[pol]\n" \
    "s_and_b32 s40, s88, 1\n" \
    "s_lshl_b32 s40, s40, 13\n" \
    "s_add_i32 s41, s40, 0x20000\n" \
    "s_add_i32 s42, s40, 0x40000\n" \
    "s_mov_b32 s96, 0\n" \
    "s_mov_b32 s90, s40\n" \
    "s_mov_b32 s43, 0\n" \
    "s_mov_b32 s70, 0\n" \
    "s_mov_b32 s44, 0\n" \
    "s_mov_b32 s56, 0\n" \
    "s_mov_b32 s53, 0\n" \
    "s_mov_b32 s74, 0\n" \
    "s_mov_b32 s68, 12\n" \
    "s_mov_b32 s85, 8\n" \
    "s_mov_b32 s86, 0\n" \
    "s_mov_b32 s87, 0\n" \
    "s_add_i32 s84, s40, 0x4000\n" \
    "s_mov_b32 s92, 1520\n" \
    "s_mov_b32 s93, 848\n" \
    "s_mov_b32 s94, 0\n" \
    "s_mov_b32 s95, 0\n" \
    "s_cmp_eq_u32 %[w], 0\n" \
    "s_cbranch_scc1 ow_main%=\n" \
    "s_cmp_eq_u32 %[w], 2\n" \
    "s_cbranch_scc1 ow_arch%=\n" \
    "s_lshr_b32 s48, %[w], 1\n" \
    "s_lshl_b32 s48, s48, 14\n" \
    "v_mov_b32 v46, %[fv]\n" \
    "v_add_u32 v47, s48, %[l16]\n" \
    "s_lshl_b32 s48, s48, 1\n" \
    "s_add_i32 s52, %[vox], s48\n" \
    "v_lshrrev_b32 v51, 2, %[l16]\n" \
    "v_and_b32 v51, 128, v51\n" \
    "v_add_u32 v51, v46, v51\n" \
    "s_mov_b32 s49, 1\n" \
    "s_mov_b32 s71, 0\n" \
    "ows_f%=:\n" \
    "buffer_load_dword v48, v51, %[rr], 0 offen sc1\n" \
    "s_waitcnt vmcnt(0)\n" \
    "v_cmp_gt_u32 vcc, s49, v48\n" \
    "s_cbranch_vccz ows_fk%=\n" \
    "s_sleep 6\n" \
    "s_add_i32 s71, s71, 1\n" \
    "s_cmp_lt_u32 s71, 0x40000\n" \
    "s_cbranch_scc1 ows_f%=\n" \
    "s_branch ow_fail%=\n" \
    "ows_fk%=:\n" \
    "buffer_load_dwordx4 a[0:3], %[l16], %[rr], s52 offen offset:0 sc1\n" \
    "buffer_load_dwordx4 a[4:7], %[l16], %[rr], s52 offen offset:1024 sc1\n" \
    "buffer_load_dwordx4 a[8:11], %[l16], %[rr], s52 offen offset:2048 sc1\n" \
    "buffer_load_dwordx4 a[12:15], %[l16], %[rr], s52 offen offset:3072 sc1\n" \
    "s_add_u32 s52, s52, 4096\n" \
    "buffer_load_dwordx4 a[16:19], %[l16], %[rr], s52 offen offset:0 sc1\n" \
    "buffer_load_dwordx4 a[20:23], %[l16], %[rr], s52 offen offset:1024 sc1\n" \
    "buffer_load_dwordx4 a[24:27], %[l16], %[rr], s52 offen offset:2048 sc1\n" \
    "buffer_load_dwordx4 a[28:31], %[l16], %[rr], s52 offen offset:3072 sc1\n" \
    "s_add_u32 s52, s52, 12288\n" \
    "buffer_load_dwordx4 a[32:35], %[l16], %[rr], s52 offen offset:0 sc1\n" \
    "buffer_load_dwordx4 a[36:39], %[l16], %[rr], s52 offen offset:1024 sc1\n" \
    "buffer_load_dwordx4 a[40:43], %[l16], %[rr], s52 offen offset:2048 sc1\n" \
    "buffer_load_dwordx4 a[44:47], %[l16], %[rr], s52 offen offset:3072 sc1\n" \
    "s_add_u32 s52, s52, 4096\n" \
    "buffer_load_dwordx4 a[48:51], %[l16], %[rr], s52 offen offset:0 sc1\n" \
    "buffer_load_dwordx4 a[52:55], %[l16], %[rr], s52 offen offset:1024 sc1\n" \
    "buffer_load_dwordx4 a[56:59], %[l16], %[rr], s52 offen offset:2048 sc1\n" \
    "buffer_load_dwordx4 a[60:63], %[l16], %[rr], s52 offen offset:3072 sc1\n" \
    "s_add_u32 s52, s52, 12288\n" \
    "s_add_u32 s52, s52, 0x1F8000\n" \
    "s_xor_b32 s52, s52, 0x2000\n" \
    "s_waitcnt vmcnt(15)\n" \
    "ds_write_b128 v47, a[0:3] offset:0\n" \
    "s_waitcnt vmcnt(14)\n" \
    "ds_write_b128 v47, a[4:7] offset:1024\n" \
    "s_waitcnt vmcnt(13)\n" \
    "ds_write_b128 v47, a[8:11] offset:2048\n" \
    "s_waitcnt vmcnt(12)\n" \
    "ds_write_b128 v47, a[12:15] offset:3072\n" \
    "s_waitcnt vmcnt(11)\n" \
    "ds_write_b128 v47, a[16:19] offset:4096\n" \
    "s_waitcnt vmcnt(10)\n" \
    "ds_write_b128 v47, a[20:23] offset:5120\n" \
    "s_waitcnt vmcnt(9)\n" \
    "ds_write_b128 v47, a[24:27] offset:6144\n" \
    "s_waitcnt vmcnt(8)\n" \
    "ds_write_b128 v47, a[28:31] offset:7168\n" \
    "buffer_load_dwordx4 a[64:67], %[l16], %[rr], s52 offen offset:0 sc1\n" \
    "buffer_load_dwordx4 a[68:71], %[l16], %[rr], s52 offen offset:1024 sc1\n" \
    "buffer_load_dwordx4 a[72:75], %[l16], %[rr], s52 offen offset:2048 sc1\n" \
    "buffer_load_dwordx4 a[76:79], %[l16], %[rr], s52 offen offset:3072 sc1\n" \
    "s_add_u32 s52, s52, 4096\n" \
    "buffer_load_dwordx4 a[80:83], %[l16], %[rr], s52 offen offset:0 sc1\n" \
    "buffer_load_dwordx4 a[84:87], %[l16], %[rr], s52 offen offset:1024 sc1\n" \
    "buffer_load_dwordx4 a[88:91], %[l16], %[rr], s52 offen offset:2048 sc1\n" \
    "buffer_load_dwordx4 a[92:95], %[l16], %[rr], s52 offen offset:3072 sc1\n" \
    "s_add_u32 s52, s52, 12288\n" \
    "buffer_load_dwordx4 a[96:99], %[l16], %[rr], s52 offen offset:0 sc1\n" \
    "buffer_load_dwordx4 a[100:103], %[l16], %[rr], s52 offen offset:1024 sc1\n" \
    "buffer_load_dwordx4 a[104:107], %[l16], %[rr], s52 offen offset:2048 sc1\n" \
    "buffer_load_dwordx4 a[108:111], %[l16], %[rr], s52 offen offset:3072 sc1\n" \
    "s_add_u32 s52, s52, 4096\n" \
    "buffer_load_dwordx4 a[112:115], %[l16], %[rr], s52 offen offset:0 sc1\n" \
    "buffer_load_dwordx4 a[116:119], %[l16], %[rr], s52 offen offset:1024 sc1\n" \
    "buffer_load_dwordx4 a[120:123], %[l16], %[rr], s52 offen offset:2048 sc1\n" \
    "buffer_load_dwordx4 a[124:127], %[l16], %[rr], s52 offen offset:3072 sc1\n" \
    "s_add_u32 s52, s52, 12288\n" \
    "s_add_u32 s52, s52, 0x1F8000\n" \
    "s_xor_b32 s52, s52, 0x2000\n" \
    "s_waitcnt vmcnt(23)\n" \
    "ds_write_b128 v47, a[32:35] offset:8192\n" \
    "s_waitcnt vmcnt(22)\n" \
    "ds_write_b128 v47, a[36:39] offset:9216\n" \
    "s_waitcnt vmcnt(21)\n" \
    "ds_write_b128 v47, a[40:43] offset:10240\n" \
    "s_waitcnt vmcnt(20)\n" \
    "ds_write_b128 v47, a[44:47] offset:11264\n" \
    "s_waitcnt vmcnt(19)\n" \
    "ds_write_b128 v47, a[48:51] offset:12288\n" \
    "s_waitcnt vmcnt(18)\n" \
    "ds_write_b128 v47, a[52:55] offset:13312\n" \
    "s_waitcnt vmcnt(17)\n" \
    "ds_write_b128 v47, a[56:59] offset:14336\n" \
    "s_waitcnt vmcnt(16)\n" \
    "ds_write_b128 v47, a[60:63] offset:15360\n" \
    "s_waitcnt lgkmcnt(0)\n" \
    "s_barrier\n" \
    "s_waitcnt vmcnt(15)\n" \
    "ds_write_b128 v47, a[64:67] offset:32768\n" \
    "s_waitcnt vmcnt(14)\n" \
    "ds_write_b128 v47, a[68:71] offset:33792\n" \
    "s_waitcnt vmcnt(13)\n" \
    "ds_write_b128 v47, a[72:75] offset:34816\n" \
    "s_waitcnt vmcnt(12)\n" \
    "ds_write_b128 v47, a[76:79] offset:35840\n" \
    "s_waitcnt vmcnt(11)\n" \
    "ds_write_b128 v47, a[80:83] offset:36864\n" \
    "s_waitcnt vmcnt(10)\n" \
    "ds_write_b128 v47, a[84:87] offset:37888\n" \
    "s_waitcnt vmcnt(9)\n" \
    "ds_write_b128 v47, a[88:91] offset:38912\n" \
    "s_waitcnt vmcnt(8)\n" \
    "ds_write_b128 v47, a[92:95] offset:39936\n" \
    "s_waitcnt vmcnt(7)\n" \
    "ds_write_b128 v47, a[96:99] offset:40960\n" \
    "s_waitcnt vmcnt(6)\n" \
    "ds_write_b128 v47, a[100:103] offset:41984\n" \
    "s_waitcnt vmcnt(5)\n" \
    "ds_write_b128 v47, a[104:107] offset:43008\n" \
    "s_waitcnt vmcnt(4)\n" \
    "ds_write_b128 v47, a[108:111] offset:44032\n" \
    "s_waitcnt vmcnt(3)\n" \
    "ds_write_b128 v47, a[112:115] offset:45056\n" \
    "s_waitcnt vmcnt(2)\n" \
    "ds_write_b128 v47, a[116:119] offset:46080\n" \
    "s_waitcnt vmcnt(1)\n" \
    "ds_write_b128 v47, a[120:123] offset:47104\n" \
    "s_waitcnt vmcnt(0)\n" \
    "ds_write_b128 v47, a[124:127] offset:48128\n" \
    "s_waitcnt lgkmcnt(0)\n" \
    "s_mov_b32 s44, 2\n" \
    "v_mov_b32 v48, 0\n" \
    "owp_loop%=:\n" \
    "s_lshr_b32 s49, s44, 1\n" \
    "s_add_i32 s49, s49, 1\n" \
    "s_cmp_ge_u32 s53, s49\n" \
    "s_cbranch_scc1 owc_skip%=\n" \
    "v_cmp_gt_u32 vcc, s49, v48\n" \
    "s_cbranch_vccz owc_fk%=\n" \
    "s_mov_b32 s71, 0\n" \
    "owc_f%=:\n" \
    "buffer_load_dword v48, v46, %[rr], 0 offen sc1\n" \
    "s_waitcnt vmcnt(0)\n" \
    "v_cmp_gt_u32 vcc, s49, v48\n" \
    "s_cbranch_vccz owc_fk%=\n" \
    "s_sleep 2\n" \
    "s_add_i32 s71, s71, 1\n" \
    "s_cmp_lt_u32 s71, 0x40000\n" \
    "s_cbranch_scc1 owc_f%=\n" \
    "s_branch ow_fail%=\n" \
    "owc_fk%=:\n" \
    "v_min_u32_dpp v48, v48, v48 row_ror:8 row_mask:0xf bank_mask:0xf\n" \
    "s_nop 1\n" \
    "v_min_u32_dpp v48, v48, v48 row_ror:4 row_mask:0xf bank_mask:0xf\n" \
    "s_nop 1\n" \
    "v_min_u32_dpp v48, v48, v48 row_ror:2 row_mask:0xf bank_mask:0xf\n" \
    "s_nop 1\n" \
    "v_min_u32_dpp v48, v48, v48 row_ror:1 row_mask:0xf bank_mask:0xf\n" \
    "s_nop 1\n" \
    "v_readfirstlane_b32 s53, v48\n" \
    "owc_skip%=:\n" \
    "s_mov_b32 s48, s53\n" \
    "s_mov_b32 s53, s74\n" \
    "s_mov_b32 s74, s48\n" \
    "v_xor_b32 v46, 128, v46\n" \
    "v_mov_b32 v48, 0\n" \
    "s_add_i32 s49, s44, 1\n" \
    "s_lshr_b32 s49, s49, 1\n" \
    "s_barrier\n" \
    "s_mov_b32 s48, %[dv3]\n" \
    "owp_d%=:\n" \
    "s_sleep 1\n" \
    "s_sub_u32 s48, s48, 1\n" \
    "s_cmp_gt_i32 s48, 0\n" \
    "s_cbranch_scc1 owp_d%=\n" \
    "s_cmp_gt_u32 s53, s49\n" \
    "s_cbranch_scc1 owp_ld%=\n" \
    "buffer_load_dword v48, v46, %[rr], 0 offen sc1\n" \
    "owp_ld%=:\n" \
    "buffer_load_dwordx4 a[0:3], %[l16], %[rr], s52 offen offset:0 sc1\n" \
    "buffer_load_dwordx4 a[4:7], %[l16], %[rr], s52 offen offset:1024 sc1\n" \
    "buffer_load_dwordx4 a[8:11], %[l16], %[rr], s52 offen offset:2048 sc1\n" \
    "buffer_load_dwordx4 a[12:15], %[l16], %[rr], s52 offen offset:3072 sc1\n" \
    "s_add_u32 s52, s52, 4096\n" \
    "buffer_load_dwordx4 a[16:19], %[l16], %[rr], s52 offen offset:0 sc1\n" \
    "buffer_load_dwordx4 a[20:23], %[l16], %[rr], s52 offen offset:1024 sc1\n" \
    "buffer_load_dwordx4 a[24:27], %[l16], %[rr], s52 offen offset:2048 sc1\n" \
    "buffer_load_dwordx4 a[28:31], %[l16], %[rr], s52 offen offset:3072 sc1\n" \
    "s_add_u32 s52, s52, 12288\n" \
    "buffer_load_dwordx4 a[32:35], %[l16], %[rr], s52 offen offset:0 sc1\n" \
    "buffer_load_dwordx4 a[36:39], %[l16], %[rr], s52 offen offset:1024 sc1\n" \
    "buffer_load_dwordx4 a[40:43], %[l16], %[rr], s52 offen offset:2048 sc1\n" \
    "buffer_load_dwordx4 a[44:47], %[l16], %[rr], s52 offen offset:3072 sc1\n" \
    "s_add_u32 s52, s52, 4096\n" \
    "buffer_load_dwordx4 a[48:51], %[l16], %[rr], s52 offen offset:0 sc1\n" \
    "buffer_load_dwordx4 a[52:55], %[l16], %[rr], s52 offen offset:1024 sc1\n" \
    "buffer_load_dwordx4 a[56:59], %[l16], %[rr], s52 offen offset:2048 sc1\n" \
    "buffer_load_dwordx4 a[60:63], %[l16], %[rr], s52 offen offset:3072 sc1\n" \
    "s_add_u32 s52, s52, 12288\n" \
    "s_add_u32 s52, s52, 0x1F8000\n" \
    "s_cmp_eq_u32 s44, 3\n" \
    "s_cselect_b32 s48, 0, 0x2000\n" \
    "s_xor_b32 s52, s52, s48\n" \
    "s_waitcnt vmcnt(15)\n" \
    "ds_write_b128 v47, a[0:3] offset:0\n" \
    "s_waitcnt vmcnt(14)\n" \
    "ds_write_b128 v47, a[4:7] offset:1024\n" \
    "s_waitcnt vmcnt(13)\n" \
    "ds_write_b128 v47, a[8:11] offset:2048\n" \
    "s_waitcnt vmcnt(12)\n" \
    "ds_write_b128 v47, a[12:15] offset:3072\n" \
    "s_waitcnt vmcnt(11)\n" \
    "ds_write_b128 v47, a[16:19] offset:4096\n" \
    "s_waitcnt vmcnt(10)\n" \
    "ds_write_b128 v47, a[20:23] offset:5120\n" \
    "s_waitcnt vmcnt(9)\n" \
    "ds_write_b128 v47, a[24:27] offset:6144\n" \
    "s_waitcnt vmcnt(8)\n" \
    "ds_write_b128 v47, a[28:31] offset:7168\n" \
    "s_waitcnt vmcnt(7)\n" \
    "ds_write_b128 v47, a[32:35] offset:8192\n" \
    "s_waitcnt vmcnt(6)\n" \
    "ds_write_b128 v47, a[36:39] offset:9216\n" \
    "s_waitcnt vmcnt(5)\n" \
    "ds_write_b128 v47, a[40:43] offset:10240\n" \
    "s_waitcnt vmcnt(4)\n" \
    "ds_write_b128 v47, a[44:47] offset:11264\n" \
    "s_waitcnt vmcnt(3)\n" \
    "ds_write_b128 v47, a[48:51] offset:12288\n" \
    "s_waitcnt vmcnt(2)\n" \
    "ds_write_b128 v47, a[52:55] offset:13312\n" \
    "s_waitcnt vmcnt(1)\n" \
    "ds_write_b128 v47, a[56:59] offset:14336\n" \
    "s_waitcnt vmcnt(0)\n" \
    "ds_write_b128 v47, a[60:63] offset:15360\n" \
    "v_xor_b32 v47, 0x8000, v47\n" \
    "s_waitcnt lgkmcnt(0)\n" \
    "s_add_i32 s44, s44, 1\n" \
    "s_cmp_lt_u32 s44, 63\n" \
    "s_cbranch_scc1 owp_loop%=\n" \
    "s_branch ow_end%=\n" \
    "ow_arch%=:\n" \
    "v_add_u32 v47, 0x10000, %[l16]\n" \
    "s_mov_b32 s52, %[ua]\n" \
    "s_barrier\n" \
    "v_mov_b32 v50, 0x12000\n" \
    "s_cmp_eq_u32 %[bc], 0\n" \
    "s_cbranch_scc1 owa_loop%=\n" \
    "s_memrealtime s[78:79]\n" \
    "s_waitcnt lgkmcnt(0)\n" \
    "s_add_i32 s48, s78, %[bc]\n" \
    "s_and_b32 s48, s48, -4\n" \
    "s_or_b32 s48, s48, %[cls]\n" \
    "v_mov_b32 v48, s48\n" \
    "s_mov_b64 exec, 0xffff\n" \
    "buffer_store_dword v48, %[g1w], %[rr], 0 offen sc1\n" \
    "s_mov_b64 exec, -1\n" \
    "owa_loop%=:\n" \
    "s_barrier\n" \
    "s_sleep 4\n" \
    "s_mov_b32 s71, 0\n" \
    "owa_s%=:\n" \
    "ds_read_b64 v[48:49], v50\n" \
    "s_waitcnt lgkmcnt(0)\n" \
    "v_readfirstlane_b32 s48, v48\n" \
    "v_readfirstlane_b32 s72, v49\n" \
    "s_cmp_gt_u32 s48, s44\n" \
    "s_cbranch_scc1 owa_k%=\n" \
    "s_add_i32 s71, s71, 1\n" \
    "s_cmp_lt_u32 s71, 0x100000\n" \
    "s_cbranch_scc1 owa_s%=\n" \
    "s_branch ow_fail%=\n" \
    "owa_k%=:\n" \
    "ds_read_b128 v[64:67], v47 offset:0\n" \
    "ds_read_b128 v[68:71], v47 offset:1024\n" \
    "ds_read_b128 v[72:75], v47 offset:2048\n" \
    "ds_read_b128 v[76:79], v47 offset:3072\n" \
    "ds_read_b128 v[80:83], v47 offset:4096\n" \
    "ds_read_b128 v[84:87], v47 offset:5120\n" \
    "ds_read_b128 v[88:91], v47 offset:6144\n" \
    "ds_read_b128 v[92:95], v47 offset:7168\n" \
    "s_waitcnt lgkmcnt(0)\n" \
    "s_add_i32 s72, s72, %[dv]\n" \
    "owt_w%=:\n" \
    "s_memrealtime s[78:79]\n" \
    "s_getpc_b64 s[82:83]\n" \
    "owt_pc%=:\n" \
    "s_waitcnt lgkmcnt(0)\n" \
    "s_sub_i32 s48, s72, s78\n" \
    "s_add_i32 s77, s48, -4\n" \
    "s_cmp_gt_u32 s77, 2000\n" \
    "s_cbranch_scc1 owt_go%=\n" \
    "s_cmp_lt_i32 s48, 18\n" \
    "s_cbranch_scc1 owt_ns%=\n" \
    "s_sleep 2\n" \
    "s_branch owt_w%=\n" \
    "owt_ns%=:\n" \
    "s_and_b32 s48, s48, 3\n" \
    "s_sub_i32 s77, s72, s48\n" \
    "s_lshl_b32 s48, s48, 2\n" \
    "s_add_u32 s82, s82, owt_go%=-owt_pc%=\n" \
    "s_addc_u32 s83, s83, 0\n" \
    "s_sub_u32 s82, s82, s48\n" \
    "s_subb_u32 s83, s83, 0\n" \
    "s_mov_b32 s80, s78\n" \
    "owt_l%=:\n" \
    "s_memrealtime s[78:79]\n" \
    "s_waitcnt lgkmcnt(2)\n" \
    "s_sub_i32 s48, s80, s77\n" \
    "s_cmp_ge_i32 s48, 0\n" \
    "s_cbranch_scc1 owt_x%=\n" \
    "s_memrealtime s[80:81]\n" \
    "s_waitcnt lgkmcnt(2)\n" \
    "s_sub_i32 s48, s78, s77\n" \
    "s_cmp_ge_i32 s48, 0\n" \
    "s_cbranch_scc1 owt_x%=\n" \
    "s_memrealtime s[78:79]\n" \
    "s_waitcnt lgkmcnt(2)\n" \
    "s_sub_i32 s48, s80, s77\n" \
    "s_cmp_ge_i32 s48, 0\n" \
    "s_cbranch_scc1 owt_x%=\n" \
    "s_memrealtime s[80:81]\n" \
    "s_waitcnt lgkmcnt(2)\n" \
    "s_sub_i32 s48, s78, s77\n" \
    "s_cmp_ge_i32 s48, 0\n" \
    "s_cbranch_scc1 owt_x%=\n" \
    "s_memrealtime s[78:79]\n" \
    "s_waitcnt lgkmcnt(2)\n" \
    "s_sub_i32 s48, s80, s77\n" \
    "s_cmp_ge_i32 s48, 0\n" \
    "s_cbranch_scc1 owt_x%=\n" \
    "s_memrealtime s[80:81]\n" \
    "s_waitcnt lgkmcnt(2)\n" \
    "s_sub_i32 s48, s78, s77\n" \
    "s_cmp_ge_i32 s48, 0\n" \
    "s_cbranch_scc1 owt_x%=\n" \
    "s_memrealtime s[78:79]\n" \
    "s_waitcnt lgkmcnt(2)\n" \
    "s_sub_i32 s48, s80, s77\n" \
    "s_cmp_ge_i32 s48, 0\n" \
    "s_cbranch_scc1 owt_x%=\n" \
    "s_memrealtime s[80:81]\n" \
    "s_waitcnt lgkmcnt(2)\n" \
    "s_sub_i32 s48, s78, s77\n" \
    "s_cmp_ge_i32 s48, 0\n" \
    "s_cbranch_scc1 owt_x%=\n" \
    "s_branch owt_l%=\n" \
    "owt_x%=:\n" \
    "s_setpc_b64 s[82:83]\n" \
    "s_nop 5\n" \
    "s_nop 5\n" \
    "s_nop 5\n" \
    "owt_go%=:\n" \
    "s_mov_b32 s75, s52\n" \
    "buffer_store_dwordx4 v[64:67], %[l16], %[rr], s75 offen offset:0 sc1\n" \
    "buffer_store_dwordx4 v[68:71], %[l16], %[rr], s75 offen offset:1024 sc1\n" \
    "buffer_store_dwordx4 v[72:75], %[l16], %[rr], s75 offen offset:2048 sc1\n" \
    "buffer_store_dwordx4 v[76:79], %[l16], %[rr], s75 offen offset:3072 sc1\n" \
    "buffer_store_dwordx4 v[80:83], %[l16b], %[rr], s75 offen offset:0 sc1\n" \
    "buffer_store_dwordx4 v[84:87], %[l16b], %[rr], s75 offen offset:1024 sc1\n" \
    "buffer_store_dwordx4 v[88:91], %[l16b], %[rr], s75 offen offset:2048 sc1\n" \
    "buffer_store_dwordx4 v[92:95], %[l16b], %[rr], s75 offen offset:3072 sc1\n" \
    "s_add_i32 s52, s52, 0x40000\n" \
    "s_waitcnt vmcnt(0)\n" \
    "s_add_i32 s44, s44, 1\n" \
    "s_cmp_lt_u32 s44, 58\n" \
    "s_cbranch_scc1 owa_loop%=\n" \
    "v_mov_b32 v48, s44\n" \
    "s_mov_b64 exec, 1\n" \
    "buffer_store_dword v48, off, %[rr], %[far] sc1\n" \
    "s_mov_b64 exec, -1\n" \
    "s_cmp_lt_u32 s44, 61\n" \
    "s_cbranch_scc1 owa_loop%=\n" \
    "s_branch ow_end%=\n" \
    "ow_main%=:\n" \
    "v_mov_b32 v28, -1\n" \
    "v_mov_b32 v29, -1\n" \
    "v_mov_b32 v30, -1\n" \
    "v_mov_b32 v31, -1\n" \
    "v_mov_b32 v47, %[l16]\n" \
    "v_add_u32 v46, 0x10000, %[l16]\n" \
    "v_mov_b32 v50, 0x12000\n" \
    "v_mov_b32 v48, 0\n" \
    "ds_write_b32 v50, v48\n" \
    "s_waitcnt lgkmcnt(0)\n" \
    "s_mov_b32 s45, 0\n" \
    "s_mov_b32 s57, 0\n" \
    "s_add_i32 s60, s40, %[uoff]\n" \
    "s_add_i32 s61, s40, 0x8000\n" \
    "s_add_i32 s62, s40, 0x10000\n" \
    "s_add_i32 s63, s40, 0x18000\n" \
    "v_mov_b32 v32, 0\n" \
    "v_mov_b32 v33, 0\n" \
    "v_mov_b32 v34, 0\n" \
    "v_mov_b32 v35, 0\n" \
    "s_memrealtime s[78:79]\n" \
    "s_waitcnt lgkmcnt(0)\n" \
    "s_mov_b32 s57, s78\n" \
    "s_mov_b32 s70, s78\n" \
    "s_mov_b32 s71, 0\n" \
    "ow_p0%=:\n" \
    "s_sleep 4\n" \
    "buffer_load_dwordx2 v[48:49], %[sto], %[rr], s41 offen sc1\n" \
    "s_waitcnt vmcnt(0)\n" \
    "v_cmp_ne_u32 vcc, 1, v49\n" \
    "s_cmp_eq_u32 vcc_hi, 0\n" \
    "s_cbranch_scc1 ow_p1%=\n" \
    "s_add_i32 s71, s71, 1\n" \
    "s_cmp_lt_u32 s71, 0x40000\n" \
    "s_cbranch_scc1 ow_p0%=\n" \
    "s_branch ow_fail%=\n" \
    "ow_f0%=:\n" \
    "s_sleep 8\n" \
    "ow_p1%=:\n" \
    "buffer_load_dwordx4 v[96:99], %[l16], %[rr], s61 offen offset:0 sc1\n" \
    "buffer_load_dwordx4 v[100:103], %[l16], %[rr], s61 offen offset:1024 sc1\n" \
    "buffer_load_dwordx4 v[104:107], %[l16], %[rr], s61 offen offset:2048 sc1\n" \
    "buffer_load_dwordx4 v[108:111], %[l16], %[rr], s61 offen offset:3072 sc1\n" \
    "buffer_load_dwordx4 v[112:115], %[l16b], %[rr], s61 offen offset:0 sc1\n" \
    "buffer_load_dwordx4 v[116:119], %[l16b], %[rr], s61 offen offset:1024 sc1\n" \
    "buffer_load_dwordx4 v[120:123], %[l16b], %[rr], s61 offen offset:2048 sc1\n" \
    "buffer_load_dwordx4 v[124:127], %[l16b], %[rr], s61 offen offset:3072 sc1\n" \
    "buffer_load_dwordx4 v[128:131], %[l16], %[rr], s62 offen offset:0 sc1\n" \
    "buffer_load_dwordx4 v[132:135], %[l16], %[rr], s62 offen offset:1024 sc1\n" \
    "buffer_load_dwordx4 v[136:139], %[l16], %[rr], s62 offen offset:2048 sc1\n" \
    "buffer_load_dwordx4 v[140:143], %[l16], %[rr], s62 offen offset:3072 sc1\n" \
    "buffer_load_dwordx4 v[144:147], %[l16b], %[rr], s62 offen offset:0 sc1\n" \
    "buffer_load_dwordx4 v[148:151], %[l16b], %[rr], s62 offen offset:1024 sc1\n" \
    "buffer_load_dwordx4 v[152:155], %[l16b], %[rr], s62 offen offset:2048 sc1\n" \
    "buffer_load_dwordx4 v[156:159], %[l16b], %[rr], s62 offen offset:3072 sc1\n" \
    "buffer_load_dwordx4 v[160:163], %[l16], %[rr], s63 offen offset:0 sc1\n" \
    "buffer_load_dwordx4 v[164:167], %[l16], %[rr], s63 offen offset:1024 sc1\n" \
    "buffer_load_dwordx4 v[168:171], %[l16], %[rr], s63 offen offset:2048 sc1\n" \
    "buffer_load_dwordx4 v[172:175], %[l16], %[rr], s63 offen offset:3072 sc1\n" \
    "buffer_load_dwordx4 v[64:67], %[l16], %[rr], s60 offen offset:0 sc1\n" \
    "buffer_load_dwordx4 v[68:71], %[l16], %[rr], s60 offen offset:1024 sc1\n" \
    "buffer_load_dwordx4 v[72:75], %[l16], %[rr], s60 offen offset:2048 sc1\n" \
    "buffer_load_dwordx4 v[76:79], %[l16], %[rr], s60 offen offset:3072 sc1\n" \
    "buffer_load_dwordx4 v[80:83], %[l16b], %[rr], s60 offen offset:0 sc1\n" \
    "buffer_load_dwordx4 v[84:87], %[l16b], %[rr], s60 offen offset:1024 sc1\n" \
    "buffer_load_dwordx4 v[88:91], %[l16b], %[rr], s60 offen offset:2048 sc1\n" \
    "buffer_load_dwordx4 v[92:95], %[l16b], %[rr], s60 offen offset:3072 sc1\n" \
    "s_waitcnt vmcnt(0)\n" \
    "s_nop 7\n" \
    "v_max_u32 v60, v64, v65\n" \
    "v_max3_u32 v60, v60, v66, v67\n" \
    "v_max3_u32 v60, v60, v68, v69\n" \
    "v_max3_u32 v60, v60, v70, v71\n" \
    "v_max3_u32 v60, v60, v72, v73\n" \
    "v_max3_u32 v60, v60, v74, v75\n" \
    "v_max3_u32 v60, v60, v76, v77\n" \
    "v_max3_u32 v60, v60, v78, v79\n" \
    "v_max3_u32 v60, v60, v80, v81\n" \
    "v_max3_u32 v60, v60, v82, v83\n" \
    "v_max3_u32 v60, v60, v84, v85\n" \
    "v_max3_u32 v60, v60, v86, v87\n" \
    "v_max3_u32 v60, v60, v88, v89\n" \
    "v_max3_u32 v60, v60, v90, v91\n" \
    "v_max3_u32 v60, v60, v92, v93\n" \
    "v_max3_u32 v60, v60, v94, v95\n" \
    "v_max3_u32 v60, v60, v96, v97\n" \
    "v_max3_u32 v60, v60, v98, v99\n" \
    "v_max3_u32 v60, v60, v100, v101\n" \
    "v_max3_u32 v60, v60, v102, v103\n" \
    "v_max3_u32 v60, v60, v104, v105\n" \
    "v_max3_u32 v60, v60, v106, v107\n" \
    "v_max3_u32 v60, v60, v108, v109\n" \
    "v_max3_u32 v60, v60, v110, v111\n" \
    "v_max3_u32 v60, v60, v112, v113\n" \
    "v_max3_u32 v60, v60, v114, v115\n" \
    "v_max3_u32 v60, v60, v116, v117\n" \
    "v_max3_u32 v60, v60, v118, v119\n" \
    "v_max3_u32 v60, v60, v120, v121\n" \
    "v_max3_u32 v60, v60, v122, v123\n" \
    "v_max3_u32 v60, v60, v124, v125\n" \
    "v_max3_u32 v60, v60, v126, v127\n" \
    "v_max3_u32 v60, v60, v128, v129\n" \
    "v_max3_u32 v60, v60, v130, v131\n" \
    "v_max3_u32 v60, v60, v132, v133\n" \
    "v_max3_u32 v60, v60, v134, v135\n" \
    "v_max3_u32 v60, v60, v136, v137\n" \
    "v_max3_u32 v60, v60, v138, v139\n" \
    "v_max3_u32 v60, v60, v140, v141\n" \
    "v_max3_u32 v60, v60, v142, v143\n" \
    "v_max3_u32 v60, v60, v144, v145\n" \
    "v_max3_u32 v60, v60, v146, v147\n" \
    "v_max3_u32 v60, v60, v148, v149\n" \
    "v_max3_u32 v60, v60, v150, v151\n" \
    "v_max3_u32 v60, v60, v152, v153\n" \
    "v_max3_u32 v60, v60, v154, v155\n" \
    "v_max3_u32 v60, v60, v156, v157\n" \
    "v_max3_u32 v60, v60, v158, v159\n" \
    "v_max3_u32 v60, v60, v160, v161\n" \
    "v_max3_u32 v60, v60, v162, v163\n" \
    "v_max3_u32 v60, v60, v164, v165\n" \
    "v_max3_u32 v60, v60, v166, v167\n" \
    "v_max3_u32 v60, v60, v168, v169\n" \
    "v_max3_u32 v60, v60, v170, v171\n" \
    "v_max3_u32 v60, v60, v172, v173\n" \
    "v_max3_u32 v60, v60, v174, v175\n" \
    "v_cmp_eq_u32 vcc, -1, v60\n" \
    "s_cbranch_vccz ow_f1%=\n" \
    "s_add_i32 s45, s45, 1\n" \
    "s_cmp_lt_u32 s45, 0x40000\n" \
    "s_cbranch_scc1 ow_f0%=\n" \
    "s_branch ow_fail%=\n" \
    "ow_f1%=:\n" \
    "s_mov_b32 s45, 3\n" \
    "s_barrier\n" \
    "s_branch ow_rd%=\n" \
    "ow_loop%=:\n" \
    "s_mov_b32 s45, 0\n" \
    "s_cmp_eq_u32 s44, 1\n" \
    "s_cbranch_scc0 owz_n0%=\n" \
    "s_mov_b32 s71, 0\n" \
    "owz_g1%=:\n" \
    "buffer_load_dword v48, off, %[rr], %[g1] sc1\n" \
    "s_waitcnt vmcnt(0)\n" \
    "v_readfirstlane_b32 s43, v48\n" \
    "s_xor_b32 s96, s43, %[odd]\n" \
    "s_and_b32 s96, s96, 2\n" \
    "s_lshl_b32 s96, s96, 13\n" \
    "s_cmp_lg_u32 s43, 0\n" \
    "s_cbranch_scc1 owz_n0%=\n" \
    "s_sleep 3\n" \
    "s_add_i32 s71, s71, 1\n" \
    "s_cmp_lt_u32 s71, 0x40000\n" \
    "s_cbranch_scc1 owz_g1%=\n" \
    "s_branch owz_fail%=\n" \
    "owz_fail%=:\n" \
    "s_branch ow_fail%=\n" \
    "owz_n0%=:\n" \
    "s_add_i32 s90, s40, s96\n" \
    "s_add_i32 s72, s43, %[og]\n" \
    "owq_w%=:\n" \
    "s_sleep 1\n" \
    "s_memrealtime s[78:79]\n" \
    "s_waitcnt lgkmcnt(0)\n" \
    "s_sub_i32 s48, s72, s78\n" \
    "s_add_i32 s48, s48, -1\n" \
    "s_cmp_lt_u32 s48, 2000\n" \
    "s_cbranch_scc1 owq_w%=\n" \
    "buffer_load_dwordx2 v[54:55], %[sto], %[rr], s40 offen sc1\n" \
    "ow_rd%=:\n" \
    "ds_read_b128 a[0:3], v47 offset:0\n" \
    "ds_read_b128 a[4:7], v47 offset:1024\n" \
    "ds_read_b128 a[8:11], v47 offset:2048\n" \
    "ds_read_b128 a[12:15], v47 offset:3072\n" \
    "ds_read_b128 a[16:19], v47 offset:4096\n" \
    "ds_read_b128 a[20:23], v47 offset:5120\n" \
    "ds_read_b128 a[24:27], v47 offset:6144\n" \
    "ds_read_b128 a[28:31], v47 offset:7168\n" \
    "s_waitcnt lgkmcnt(0)\n" \
    "ds_read_b128 a[32:35], v47 offset:8192\n" \
    "ds_read_b128 a[36:39], v47 offset:9216\n" \
    "ds_read_b128 a[40:43], v47 offset:10240\n" \
    "ds_read_b128 a[44:47], v47 offset:11264\n" \
    "ds_read_b128 a[48:51], v47 offset:12288\n" \
    "ds_read_b128 a[52:55], v47 offset:13312\n" \
    "ds_read_b128 a[56:59], v47 offset:14336\n" \
    "ds_read_b128 a[60:63], v47 offset:15360\n" \
    "s_waitcnt lgkmcnt(0)\n" \
    "ds_read_b128 a[64:67], v47 offset:16384\n" \
    "ds_read_b128 a[68:71], v47 offset:17408\n" \
    "ds_read_b128 a[72:75], v47 offset:18432\n" \
    "ds_read_b128 a[76:79], v47 offset:19456\n" \
    "ds_read_b128 a[80:83], v47 offset:20480\n" \
    "ds_read_b128 a[84:87], v47 offset:21504\n" \
    "ds_read_b128 a[88:91], v47 offset:22528\n" \
    "ds_read_b128 a[92:95], v47 offset:23552\n" \
    "s_waitcnt lgkmcnt(0)\n" \
    "ds_read_b128 a[96:99], v47 offset:24576\n" \
    "ds_read_b128 a[100:103], v47 offset:25600\n" \
    "ds_read_b128 a[104:107], v47 offset:26624\n" \
    "ds_read_b128 a[108:111], v47 offset:27648\n" \
    "ds_read_b128 a[112:115], v47 offset:28672\n" \
    "ds_read_b128 a[116:119], v47 offset:29696\n" \
    "ds_read_b128 a[120:123], v47 offset:30720\n" \
    "ds_read_b128 a[124:127], v47 offset:31744\n" \
    "s_waitcnt lgkmcnt(0)\n" \
    "v_xor_b32 v47, 0x8000, v47\n" \
    "s_cmp_eq_u32 s44, 0\n" \
    "s_cbranch_scc1 ow_mm%=\n" \
    "s_waitcnt vmcnt(0)\n" \
    "s_cmp_eq_u32 s44, 0\n" \
    "s_cbranch_scc1 ow_n0%=\n" \
    "s_mov_b32 s71, 0\n" \
    "v_mov_b32 v48, v54\n" \
    "v_mov_b32 v49, v55\n" \
    "s_branch owb_tc%=\n" \
    "owb_t%=:\n" \
    "buffer_load_dwordx2 v[48:49], %[sto], %[rr], s40 offen sc1\n" \
    "s_waitcnt vmcnt(0)\n" \
    "owb_tc%=:\n" \
    "v_and_b32 v51, 0xfffffeff, v49\n" \
    "v_cmp_ne_u32 vcc, s44, v51\n" \
    "s_cbranch_vccz owb_tk%=\n" \
    "s_sleep 2\n" \
    "s_add_i32 s71, s71, 1\n" \
    "s_cmp_lt_u32 s71, 0x40000\n" \
    "s_cbranch_scc1 owb_t%=\n" \
    "s_branch ow_fail%=\n" \
    "owb_tk%=:\n" \
    "v_cmp_ne_u32 vcc, v51, v49\n" \
    "v_readfirstlane_b32 s70, v48\n" \
    "s_mov_b32 s69, vcc_lo\n" \
    "s_nop 0\n" \
    "v_subrev_u32 v48, s70, v48\n" \
    "s_nop 1\n" \
    "v_max_i32_dpp v48, v48, v48 row_ror:8 row_mask:0xf bank_mask:0xf\n" \
    "s_nop 1\n" \
    "v_max_i32_dpp v48, v48, v48 row_ror:4 row_mask:0xf bank_mask:0xf\n" \
    "s_nop 1\n" \
    "v_max_i32_dpp v48, v48, v48 row_ror:2 row_mask:0xf bank_mask:0xf\n" \
    "s_nop 1\n" \
    "v_max_i32_dpp v48, v48, v48 row_ror:1 row_mask:0xf bank_mask:0xf\n" \
    "s_nop 1\n" \
    "v_readlane_b32 s91, v48, 32\n" \
    "v_readlane_b32 s48, v48, 48\n" \
    "s_max_i32 s91, s91, s48\n" \
    "s_add_i32 s91, s91, s70\n" \
    "v_readlane_b32 s71, v48, 0\n" \
    "v_readlane_b32 s48, v48, 16\n" \
    "s_max_i32 s71, s71, s48\n" \
    "s_add_i32 s70, s70, s71\n" \
    "s_sub_i32 s71, s91, s94\n" \
    "s_lshl_b32 s71, s71, 4\n" \
    "s_sub_i32 s71, s71, s92\n" \
    "s_add_i32 s73, s71, 160\n" \
    "s_cmp_lt_u32 s73, 320\n" \
    "s_cselect_b32 s71, s71, 0\n" \
    "s_ashr_i32 s71, s71, 2\n" \
    "s_add_i32 s92, s92, s71\n" \
    "s_sub_i32 s71, s70, s95\n" \
    "s_lshl_b32 s71, s71, 4\n" \
    "s_sub_i32 s71, s71, s93\n" \
    "s_add_i32 s73, s71, 160\n" \
    "s_cmp_lt_u32 s73, 288\n" \
    "s_cselect_b32 s71, s71, 0\n" \
    "s_ashr_i32 s71, s71, 2\n" \
    "s_add_i32 s93, s93, s71\n" \
    "s_add_i32 s48, s70, %[V]\n" \
    "s_sub_i32 s48, s48, s43\n" \
    "s_add_i32 s70, s70, %[V2]\n" \
    "s_cmp_eq_u32 s44, 1\n" \
    "s_cselect_b32 s69, 0, s69\n" \
    "s_add_i32 s49, s48, 16\n" \
    "s_lshl_b32 s71, %[RT], 1\n" \
    "s_add_i32 s73, s43, s71\n" \
    "s_cmp_le_i32 s49, s71\n" \
    "s_cselect_b32 s70, s73, s70\n" \
    "s_add_i32 s73, s43, %[RT]\n" \
    "s_cmp_le_i32 s49, %[RT]\n" \
    "s_cselect_b32 s70, s73, s70\n" \
    "s_add_i32 s70, s70, %[XM]\n" \
    "s_add_i32 s73, s43, 22\n" \
    "s_cmp_le_i32 s48, 0\n" \
    "s_cselect_b32 s70, s73, s70\n" \
    "s_cmp_le_i32 s48, 0\n" \
    "s_cselect_b32 s70, s43, s70\n" \
    "s_cmp_lg_u32 s69, 0\n" \
    "s_cselect_b32 s71, 3, 0\n" \
    "s_add_i32 s86, s86, s71\n" \
    "s_min_i32 s86, s86, 24\n" \
    "s_cmp_lg_u32 s69, 0\n" \
    "s_cselect_b32 s71, 8, -4\n" \
    "s_add_i32 s68, s68, s71\n" \
    "s_max_i32 s68, s68, s86\n" \
    "s_cmp_gt_i32 s48, 0\n" \
    "s_cselect_b32 s71, 3, 0\n" \
    "s_cmp_gt_i32 s48, 8\n" \
    "s_cselect_b32 s71, 0, s71\n" \
    "s_add_i32 s87, s87, s71\n" \
    "s_min_i32 s87, s87, 24\n" \
    "s_cmp_gt_i32 s48, 0\n" \
    "s_cselect_b32 s71, 6, -4\n" \
    "s_cmp_gt_i32 s48, 32\n" \
    "s_cselect_b32 s71, 0, s71\n" \
    "s_cmp_lg_u32 s69, 0\n" \
    "s_cselect_b32 s71, 3, s71\n" \
    "s_add_i32 s85, s85, s71\n" \
    "s_max_i32 s85, s85, s87\n" \
    "s_add_i32 s94, s70, 0x10000\n" \
    "s_cmp_le_i32 s48, 0\n" \
    "s_cselect_b32 s94, s70, s94\n" \
    "s_lshl_b32 s71, s68, 2\n" \
    "s_add_i32 s71, s71, s92\n" \
    "s_add_i32 s71, s71, %[A]\n" \
    "s_lshr_b32 s71, s71, 4\n" \
    "s_add_i32 s95, s70, s71\n" \
    "s_lshl_b32 s71, s44, 3\n" \
    "s_min_u32 s71, s71, 31\n" \
    "s_lshr_b32 s71, %[e1], s71\n" \
    "s_and_b32 s71, s71, 0xff\n" \
    "s_add_i32 s95, s95, s71\n" \
    "s_lshl_b32 s71, s85, 2\n" \
    "s_add_i32 s71, s71, s93\n" \
    "s_add_i32 s71, s71, %[per]\n" \
    "s_lshr_b32 s71, s71, 4\n" \
    "s_add_i32 s43, s95, s71\n" \
    "ow_n0%=:\n" \
    "s_mov_b32 s57, s95\n" \
    "s_and_b32 s60, s40, 0x60000\n" \
    "s_lshl_b32 s60, s60, 2\n" \
    "s_and_b32 s48, s40, 0x2000\n" \
    "s_add_i32 s60, s60, s48\n" \
    "s_add_i32 s60, s60, %[uoff]\n" \
    "s_add_i32 s61, s90, 0x8000\n" \
    "s_add_i32 s62, s90, 0x10000\n" \
    "s_add_i32 s63, s90, 0x18000\n" \
    "s_branch ow_first%=\n" \
    "ow_retry%=:\n" \
    "s_sleep 4\n" \
    "ow_first%=:\n" \
    "v_mov_b32 v32, 0\n" \
    "v_mov_b32 v33, 0\n" \
    "v_mov_b32 v34, 0\n" \
    "v_mov_b32 v35, 0\n" \
    "s_waitcnt vmcnt(0)\n" \
    "ow_w%=:\n" \
    "s_memrealtime s[78:79]\n" \
    "s_getpc_b64 s[82:83]\n" \
    "ow_pc%=:\n" \
    "s_waitcnt lgkmcnt(0)\n" \
    "s_sub_i32 s48, s57, s78\n" \
    "s_add_i32 s77, s48, -4\n" \
    "s_cmp_gt_u32 s77, 2000\n" \
    "s_cbranch_scc1 ow_go%=\n" \
    "s_cmp_lt_i32 s48, 18\n" \
    "s_cbranch_scc1 ow_ns%=\n" \
    "s_sleep 2\n" \
    "s_branch ow_w%=\n" \
    "ow_ns%=:\n" \
    "s_and_b32 s48, s48, 3\n" \
    "s_sub_i32 s77, s57, s48\n" \
    "s_lshl_b32 s48, s48, 2\n" \
    "s_add_u32 s82, s82, ow_go%=-ow_pc%=\n" \
    "s_addc_u32 s83, s83, 0\n" \
    "s_sub_u32 s82, s82, s48\n" \
    "s_subb_u32 s83, s83, 0\n" \
    "s_mov_b32 s80, s78\n" \
    "ow_l%=:\n" \
    "s_memrealtime s[78:79]\n" \
    "s_waitcnt lgkmcnt(2)\n" \
    "s_sub_i32 s48, s80, s77\n" \
    "s_cmp_ge_i32 s48, 0\n" \
    "s_cbranch_scc1 ow_x%=\n" \
    "s_memrealtime s[80:81]\n" \
    "s_waitcnt lgkmcnt(2)\n" \
    "s_sub_i32 s48, s78, s77\n" \
    "s_cmp_ge_i32 s48, 0\n" \
    "s_cbranch_scc1 ow_x%=\n" \
    "s_memrealtime s[78:79]\n" \
    "s_waitcnt lgkmcnt(2)\n" \
    "s_sub_i32 s48, s80, s77\n" \
    "s_cmp_ge_i32 s48, 0\n" \
    "s_cbranch_scc1 ow_x%=\n" \
    "s_memrealtime s[80:81]\n" \
    "s_waitcnt lgkmcnt(2)\n" \
    "s_sub_i32 s48, s78, s77\n" \
    "s_cmp_ge_i32 s48, 0\n" \
    "s_cbranch_scc1 ow_x%=\n" \
    "s_memrealtime s[78:79]\n" \
    "s_waitcnt lgkmcnt(2)\n" \
    "s_sub_i32 s48, s80, s77\n" \
    "s_cmp_ge_i32 s48, 0\n" \
    "s_cbranch_scc1 ow_x%=\n" \
    "s_memrealtime s[80:81]\n" \
    "s_waitcnt lgkmcnt(2)\n" \
    "s_sub_i32 s48, s78, s77\n" \
    "s_cmp_ge_i32 s48, 0\n" \
    "s_cbranch_scc1 ow_x%=\n" \
    "s_memrealtime s[78:79]\n" \
    "s_waitcnt lgkmcnt(2)\n" \
    "s_sub_i32 s48, s80, s77\n" \
    "s_cmp_ge_i32 s48, 0\n" \
    "s_cbranch_scc1 ow_x%=\n" \
    "s_memrealtime s[80:81]\n" \
    "s_waitcnt lgkmcnt(2)\n" \
    "s_sub_i32 s48, s78, s77\n" \
    "s_cmp_ge_i32 s48, 0\n" \
    "s_cbranch_scc1 ow_x%=\n" \
    "s_branch ow_l%=\n" \
    "ow_x%=:\n" \
    "s_setpc_b64 s[82:83]\n" \
    "s_nop 5\n" \
    "s_nop 5\n" \
    "s_nop 5\n" \
    "ow_go%=:\n" \
    "buffer_load_dwordx4 v[96:99], %[l16], %[rr], s61 offen offset:0 sc1\n" \
    "buffer_load_dwordx4 v[100:103], %[l16], %[rr], s61 offen offset:1024 sc1\n" \
    "buffer_load_dwordx4 v[104:107], %[l16], %[rr], s61 offen offset:2048 sc1\n" \
    "buffer_load_dwordx4 v[108:111], %[l16], %[rr], s61 offen offset:3072 sc1\n" \
    "buffer_load_dwordx4 v[112:115], %[l16b], %[rr], s61 offen offset:0 sc1\n" \
    "buffer_load_dwordx4 v[116:119], %[l16b], %[rr], s61 offen offset:1024 sc1\n" \
    "buffer_load_dwordx4 v[120:123], %[l16b], %[rr], s61 offen offset:2048 sc1\n" \
    "buffer_load_dwordx4 v[124:127], %[l16b], %[rr], s61 offen offset:3072 sc1\n" \
    "buffer_load_dwordx4 v[128:131], %[l16], %[rr], s62 offen offset:0 sc1\n" \
    "buffer_load_dwordx4 v[132:135], %[l16], %[rr], s62 offen offset:1024 sc1\n" \
    "buffer_load_dwordx4 v[136:139], %[l16], %[rr], s62 offen offset:2048 sc1\n" \
    "buffer_load_dwordx4 v[140:143], %[l16], %[rr], s62 offen offset:3072 sc1\n" \
    "buffer_load_dwordx4 v[144:147], %[l16b], %[rr], s62 offen offset:0 sc1\n" \
    "buffer_load_dwordx4 v[148:151], %[l16b], %[rr], s62 offen offset:1024 sc1\n" \
    "buffer_load_dwordx4 v[152:155], %[l16b], %[rr], s62 offen offset:2048 sc1\n" \
    "buffer_load_dwordx4 v[156:159], %[l16b], %[rr], s62 offen offset:3072 sc1\n" \
    "buffer_load_dwordx4 v[64:67], %[l16], %[rr], s60 offen offset:0 sc1\n" \
    "buffer_load_dwordx4 v[68:71], %[l16], %[rr], s60 offen offset:1024 sc1\n" \
    "buffer_load_dwordx4 v[72:75], %[l16], %[rr], s60 offen offset:2048 sc1\n" \
    "buffer_load_dwordx4 v[76:79], %[l16], %[rr], s60 offen offset:3072 sc1\n" \
    "buffer_load_dwordx4 v[80:83], %[l16b], %[rr], s60 offen offset:0 sc1\n" \
    "buffer_load_dwordx4 v[84:87], %[l16b], %[rr], s60 offen offset:1024 sc1\n" \
    "buffer_load_dwordx4 v[88:91], %[l16b], %[rr], s60 offen offset:2048 sc1\n" \
    "buffer_load_dwordx4 v[92:95], %[l16b], %[rr], s60 offen offset:3072 sc1\n" \
    "buffer_load_dwordx4 v[160:163], %[l16], %[rr], s63 offen offset:0 sc1\n" \
    "buffer_load_dwordx4 v[164:167], %[l16], %[rr], s63 offen offset:1024 sc1\n" \
    "buffer_load_dwordx4 v[168:171], %[l16], %[rr], s63 offen offset:2048 sc1\n" \
    "buffer_load_dwordx4 v[172:175], %[l16], %[rr], s63 offen offset:3072 sc1\n" \
    "ow_mm%=:\n" \
    "s_waitcnt vmcnt(27)\n" \
    "v_smfmac_f32_16x16x64_bf16 v[32:35], v[96:99], a[0:7], %[sidx]\n" \
    "s_waitcnt vmcnt(26)\n" \
    "v_smfmac_f32_16x16x64_bf16 v[32:35], v[100:103], a[8:15], %[sidx]\n" \
    "s_waitcnt vmcnt(25)\n" \
    "v_smfmac_f32_16x16x64_bf16 v[32:35], v[104:107], a[16:23], %[sidx]\n" \
    "s_waitcnt vmcnt(24)\n" \
    "v_smfmac_f32_16x16x64_bf16 v[32:35], v[108:111], a[24:31], %[sidx]\n" \
    "s_waitcnt vmcnt(23)\n" \
    "v_smfmac_f32_16x16x64_bf16 v[32:35], v[112:115], a[32:39], %[sidx]\n" \
    "s_waitcnt vmcnt(22)\n" \
    "v_smfmac_f32_16x16x64_bf16 v[32:35], v[116:119], a[40:47], %[sidx]\n" \
    "s_waitcnt vmcnt(21)\n" \
    "v_smfmac_f32_16x16x64_bf16 v[32:35], v[120:123], a[48:55], %[sidx]\n" \
    "s_waitcnt vmcnt(20)\n" \
    "v_smfmac_f32_16x16x64_bf16 v[32:35], v[124:127], a[56:63], %[sidx]\n" \
    "s_waitcnt vmcnt(19)\n" \
    "v_smfmac_f32_16x16x64_bf16 v[32:35], v[128:131], a[64:71], %[sidx]\n" \
    "s_waitcnt vmcnt(18)\n" \
    "v_smfmac_f32_16x16x64_bf16 v[32:35], v[132:135], a[72:79], %[sidx]\n" \
    "s_waitcnt vmcnt(17)\n" \
    "v_smfmac_f32_16x16x64_bf16 v[32:35], v[136:139], a[80:87], %[sidx]\n" \
    "s_waitcnt vmcnt(16)\n" \
    "v_smfmac_f32_16x16x64_bf16 v[32:35], v[140:143], a[88:95], %[sidx]\n" \
    "s_waitcnt vmcnt(15)\n" \
    "v_smfmac_f32_16x16x64_bf16 v[32:35], v[144:147], a[96:103], %[sidx]\n" \
    "s_waitcnt vmcnt(14)\n" \
    "v_smfmac_f32_16x16x64_bf16 v[32:35], v[148:151], a[104:111], %[sidx]\n" \
    "s_waitcnt vmcnt(13)\n" \
    "v_smfmac_f32_16x16x64_bf16 v[32:35], v[152:155], a[112:119], %[sidx]\n" \
    "s_waitcnt vmcnt(12)\n" \
    "v_smfmac_f32_16x16x64_bf16 v[32:35], v[156:159], a[120:127], %[sidx]\n" \
    "s_waitcnt vmcnt(3)\n" \
    "v_smfmac_f32_16x16x64_bf16 v[32:35], v[160:163], v[64:71], %[sidx]\n" \
    "s_waitcnt vmcnt(2)\n" \
    "v_smfmac_f32_16x16x64_bf16 v[32:35], v[164:167], v[72:79], %[sidx]\n" \
    "s_waitcnt vmcnt(1)\n" \
    "v_smfmac_f32_16x16x64_bf16 v[32:35], v[168:171], v[80:87], %[sidx]\n" \
    "s_waitcnt vmcnt(0)\n" \
    "v_smfmac_f32_16x16x64_bf16 v[32:35], v[172:175], v[88:95], %[sidx]\n" \
    "s_nop 7\n" \
    "v_pk_add_f32 v[32:33], v[32:33], v[34:35]\n" \
    "v_cvt_pk_bf16_f32 v42, v32, v33\n" \
    "v_cmp_u_f32 vcc, v32, v33\n" \
    "s_cbranch_vccnz ow_slow%=\n" \
    "ow_ok%=:\n" \
    "v_mov_b32_dpp v43, v42 quad_perm:[1,0,3,2] row_mask:0xf bank_mask:0xf\n" \
    "v_perm_b32 v42, v43, v42, %[sel]\n" \
    "buffer_store_dword v42, %[mo], %[rr], s41 offen sc1\n" \
    "buffer_store_dword v42, %[mo2], %[rr], s41 offen sc1\n" \
    "s_memrealtime s[46:47]\n" \
    "s_mov_b32 exec_hi, 0\n" \
    "buffer_store_dwordx4 v[28:31], %[mz], %[rr], s40 offen sc1\n" \
    "s_add_i32 s48, s44, 1\n" \
    "s_cmp_lg_u32 s45, 0\n" \
    "s_cselect_b32 s49, 0x100, 0\n" \
    "s_or_b32 s49, s49, s48\n" \
    "s_waitcnt lgkmcnt(0)\n" \
    "v_mov_b32 v44, s46\n" \
    "v_mov_b32 v45, s49\n" \
    "s_mov_b64 exec, 1\n" \
    "buffer_store_dwordx2 v[44:45], %[stw], %[rr], s41 offen sc1\n" \
    "s_mov_b64 exec, -1\n" \
    "s_barrier\n" \
    "ds_write_b128 v46, v[64:67] offset:0\n" \
    "ds_write_b128 v46, v[68:71] offset:1024\n" \
    "ds_write_b128 v46, v[72:75] offset:2048\n" \
    "ds_write_b128 v46, v[76:79] offset:3072\n" \
    "ds_write_b128 v46, v[80:83] offset:4096\n" \
    "ds_write_b128 v46, v[84:87] offset:5120\n" \
    "ds_write_b128 v46, v[88:91] offset:6144\n" \
    "ds_write_b128 v46, v[92:95] offset:7168\n" \
    "v_mov_b32 v48, s48\n" \
    "v_mov_b32 v49, s43\n" \
    "s_waitcnt lgkmcnt(0)\n" \
    "ds_write_b64 v50, v[48:49]\n" \
    "s_mov_b32 s48, s40\n" \
    "s_mov_b32 s40, s41\n" \
    "s_mov_b32 s41, s42\n" \
    "s_mov_b32 s42, s48\n" \
    "s_add_i32 s44, s44, 1\n" \
    "s_add_i32 s90, s40, s96\n" \
    "s_add_i32 s84, s40, 0x4000\n" \
    "s_cmp_lt_u32 s44, 63\n" \
    "s_cbranch_scc1 ow_loop%=\n" \
    "s_branch ow_end%=\n" \
    "ow_slow%=:\n" \
    "s_add_i32 s45, s45, 1\n" \
    "s_cmp_lt_u32 s45, 3\n" \
    "s_cbranch_scc1 ow_first%=\n" \
    "s_nop 7\n" \
    "v_max_u32 v60, v64, v65\n" \
    "v_max3_u32 v60, v60, v66, v67\n" \
    "v_max3_u32 v60, v60, v68, v69\n" \
    "v_max3_u32 v60, v60, v70, v71\n" \
    "v_max3_u32 v60, v60, v72, v73\n" \
    "v_max3_u32 v60, v60, v74, v75\n" \
    "v_max3_u32 v60, v60, v76, v77\n" \
    "v_max3_u32 v60, v60, v78, v79\n" \
    "v_max3_u32 v60, v60, v80, v81\n" \
    "v_max3_u32 v60, v60, v82, v83\n" \
    "v_max3_u32 v60, v60, v84, v85\n" \
    "v_max3_u32 v60, v60, v86, v87\n" \
    "v_max3_u32 v60, v60, v88, v89\n" \
    "v_max3_u32 v60, v60, v90, v91\n" \
    "v_max3_u32 v60, v60, v92, v93\n" \
    "v_max3_u32 v60, v60, v94, v95\n" \
    "v_max3_u32 v60, v60, v96, v97\n" \
    "v_max3_u32 v60, v60, v98, v99\n" \
    "v_max3_u32 v60, v60, v100, v101\n" \
    "v_max3_u32 v60, v60, v102, v103\n" \
    "v_max3_u32 v60, v60, v104, v105\n" \
    "v_max3_u32 v60, v60, v106, v107\n" \
    "v_max3_u32 v60, v60, v108, v109\n" \
    "v_max3_u32 v60, v60, v110, v111\n" \
    "v_max3_u32 v60, v60, v112, v113\n" \
    "v_max3_u32 v60, v60, v114, v115\n" \
    "v_max3_u32 v60, v60, v116, v117\n" \
    "v_max3_u32 v60, v60, v118, v119\n" \
    "v_max3_u32 v60, v60, v120, v121\n" \
    "v_max3_u32 v60, v60, v122, v123\n" \
    "v_max3_u32 v60, v60, v124, v125\n" \
    "v_max3_u32 v60, v60, v126, v127\n" \
    "v_max3_u32 v60, v60, v128, v129\n" \
    "v_max3_u32 v60, v60, v130, v131\n" \
    "v_max3_u32 v60, v60, v132, v133\n" \
    "v_max3_u32 v60, v60, v134, v135\n" \
    "v_max3_u32 v60, v60, v136, v137\n" \
    "v_max3_u32 v60, v60, v138, v139\n" \
    "v_max3_u32 v60, v60, v140, v141\n" \
    "v_max3_u32 v60, v60, v142, v143\n" \
    "v_max3_u32 v60, v60, v144, v145\n" \
    "v_max3_u32 v60, v60, v146, v147\n" \
    "v_max3_u32 v60, v60, v148, v149\n" \
    "v_max3_u32 v60, v60, v150, v151\n" \
    "v_max3_u32 v60, v60, v152, v153\n" \
    "v_max3_u32 v60, v60, v154, v155\n" \
    "v_max3_u32 v60, v60, v156, v157\n" \
    "v_max3_u32 v60, v60, v158, v159\n" \
    "v_max3_u32 v60, v60, v160, v161\n" \
    "v_max3_u32 v60, v60, v162, v163\n" \
    "v_max3_u32 v60, v60, v164, v165\n" \
    "v_max3_u32 v60, v60, v166, v167\n" \
    "v_max3_u32 v60, v60, v168, v169\n" \
    "v_max3_u32 v60, v60, v170, v171\n" \
    "v_max3_u32 v60, v60, v172, v173\n" \
    "v_max3_u32 v60, v60, v174, v175\n" \
    "v_cmp_eq_u32 vcc, -1, v60\n" \
    "s_cbranch_vccz ow_acc%=\n" \
    "s_add_i32 s45, s45, 1\n" \
    "s_cmp_lt_u32 s45, 0x40000\n" \
    "s_cbranch_scc1 ow_retry%=\n" \
    "s_branch ow_fail%=\n" \
    "ow_acc%=:\n" \
    "v_and_b32 v43, 0xffff, v42\n" \
    "v_min_u32 v43, 0xfffe, v43\n" \
    "v_lshrrev_b32 v42, 16, v42\n" \
    "v_min_u32 v42, 0xfffe, v42\n" \
    "v_lshl_or_b32 v42, v42, 16, v43\n" \
    "s_nop 1\n" \
    "s_branch ow_ok%=\n" \
    "ow_fail%=:\n" \
    "s_mov_b32 s56, 1\n" \
    ".ifndef km_mark2\n" \
    "km_mark2:\n" \
    ".endif\n" \
    "ow_end%=:\n" \
    "s_waitcnt vmcnt(0) lgkmcnt(0)\n" \
    "s_mov_b32 %[fail], s56\n"
#define NSEG 4
#define CLOB_W "v0", "v1", "v2", "v3", "v4", "v5", "v6", "v7", "v8", "v9", "v10", "v11", "v12", "v13", "v14", "v15", "v16", "v17", "v18", "v19", "v20", "v21", "v22", "v23", "v24", "v25", "v26", "v27", "v28", "v29", "v30", "v31", "v32", "v33", "v34", "v35", "v36", "v37", "v38", "v39", "v40", "v41", "v42", "v43", "v44", "v45", "v46", "v47", "v48", "v49", "v50", "v51", "v52", "v53", "v54", "v55", "v56", "v57", "v58", "v59", "v60", "v61", "v62", "v63", "v64", "v65", "v66", "v67", "v68", "v69", "v70", "v71", "v72", "v73", "v74", "v75", "v76", "v77", "v78", "v79", "v80", "v81", "v82", "v83", "v84", "v85", "v86", "v87", "v88", "v89", "v90", "v91", "v92", "v93", "v94", "v95", "v96", "v97", "v98", "v99", "v100", "v101", "v102", "v103", "v104", "v105", "v106", "v107", "v108", "v109", "v110", "v111", "v112", "v113", "v114", "v115", "v116", "v117", "v118", "v119", "v120", "v121", "v122", "v123", "v124", "v125", "v126", "v127", "v128", "v129", "v130", "v131", "v132", "v133", "v134", "v135", "v136", "v137", "v138", "v139", "v140", "v141", "v142", "v143", "a0", "a1", "a2", "a3", "a4", "a5", "a6", "a7", "a8", "a9", "a10", "a11", "a12", "a13", "a14", "a15", "a16", "a17", "a18", "a19", "a20", "a21", "a22", "a23", "a24", "a25", "a26", "a27", "a28", "a29", "a30", "a31", "a32", "a33", "a34", "a35", "a36", "a37", "a38", "a39", "a40", "a41", "a42", "a43", "a44", "a45", "a46", "a47", "a48", "a49", "a50", "a51", "a52", "a53", "a54", "a55", "a56", "a57", "a58", "a59", "a60", "a61", "a62", "a63", "a64", "a65", "a66", "a67", "a68", "a69", "a70", "a71", "a72", "a73", "a74", "a75", "a76", "a77", "a78", "a79", "a80", "a81", "a82", "a83", "a84", "a85", "a86", "a87", "a88", "a89", "a90", "a91", "a92", "a93", "a94", "a95", "a96", "a97", "a98", "a99", "a100", "a101", "a102", "a103", "a104", "a105", "a106", "a107", "a108", "a109", "a110", "a111", "a112", "a113", "a114", "a115", "a116", "a117", "a118", "a119", "a120", "a121", "a122", "a123", "a124", "a125", "a126", "a127", "a128", "a129", "a130", "a131", "a132", "a133", "a134", "a135", "a136", "a137", "a138", "a139", "a140", "a141", "a142", "a143", "a144", "a145", "a146", "a147", "a148", "a149", "a150", "a151", "a152", "a153", "a154", "a155", "a156", "a157", "a158", "a159", "a160", "a161", "a162", "a163", "a164", "a165", "a166", "a167", "a168", "a169", "a170", "a171", "a172", "a173", "a174", "a175", "a176", "a177", "a178", "a179", "a180", "a181", "a182", "a183", "a184", "a185", "a186", "a187", "a188", "a189", "a190", "a191", "a192", "a193", "a194", "a195", "a196", "a197", "a198", "a199", "a200", "a201", "a202", "a203", "a204", "a205", "a206", "a207", "a208", "a209", "a210", "a211", "a212", "a213", "a214", "a215", "a216", "a217", "a218", "a219", "a220", "a221", "a222", "a223", "a224", "a225", "a226", "a227", "a228", "a229", "a230", "a231", "a232", "a233", "a234", "a235", "a236", "a237", "a238", "a239", "a240", "a241", "a242", "a243", "a244", "a245", "a246", "a247", "a248", "a249", "a250", "a251", "a252", "a253", "a254", "a255", "s40", "s41", "s42", "s43", "s44", "s45", "s46", "s47", "s48", "s49", "s50", "s51", "s52", "s53", "s54", "s55", "s56", "s57", "s58", "s59", "s60", "s61", "s62", "s63", "s64", "s65", "s66", "s67", "s68", "s69", "s70", "s71", "s72", "s73", "s74", "s75", "s76", "s77", "s78", "s79", "s80", "s81", "s82", "s83", "s84", "s85", "s86", "s87", "s88", "s89", "s90", "s91", "s92", "s93", "s94", "s95", "s96", "vcc", "scc", "memory"
#define CLOB_OW "v0", "v1", "v2", "v3", "v4", "v5", "v6", "v7", "v8", "v9", "v10", "v11", "v12", "v13", "v14", "v15", "v16", "v17", "v18", "v19", "v20", "v21", "v22", "v23", "v24", "v25", "v26", "v27", "v28", "v29", "v30", "v31", "v32", "v33", "v34", "v35", "v36", "v37", "v38", "v39", "v40", "v41", "v42", "v43", "v44", "v45", "v46", "v47", "v48", "v49", "v50", "v51", "v52", "v53", "v54", "v55", "v56", "v57", "v58", "v59", "v60", "v61", "v62", "v63", "v64", "v65", "v66", "v67", "v68", "v69", "v70", "v71", "v72", "v73", "v74", "v75", "v76", "v77", "v78", "v79", "v80", "v81", "v82", "v83", "v84", "v85", "v86", "v87", "v88", "v89", "v90", "v91", "v92", "v93", "v94", "v95", "v96", "v97", "v98", "v99", "v100", "v101", "v102", "v103", "v104", "v105", "v106", "v107", "v108", "v109", "v110", "v111", "v112", "v113", "v114", "v115", "v116", "v117", "v118", "v119", "v120", "v121", "v122", "v123", "v124", "v125", "v126", "v127", "v128", "v129", "v130", "v131", "v132", "v133", "v134", "v135", "v136", "v137", "v138", "v139", "v140", "v141", "v142", "v143", "v144", "v145", "v146", "v147", "v148", "v149", "v150", "v151", "v152", "v153", "v154", "v155", "v156", "v157", "v158", "v159", "v160", "v161", "v162", "v163", "v164", "v165", "v166", "v167", "v168", "v169", "v170", "v171", "v172", "v173", "v174", "v175", "a0", "a1", "a2", "a3", "a4", "a5", "a6", "a7", "a8", "a9", "a10", "a11", "a12", "a13", "a14", "a15", "a16", "a17", "a18", "a19", "a20", "a21", "a22", "a23", "a24", "a25", "a26", "a27", "a28", "a29", "a30", "a31", "a32", "a33", "a34", "a35", "a36", "a37", "a38", "a39", "a40", "a41", "a42", "a43", "a44", "a45", "a46", "a47", "a48", "a49", "a50", "a51", "a52", "a53", "a54", "a55", "a56", "a57", "a58", "a59", "a60", "a61", "a62", "a63", "a64", "a65", "a66", "a67", "a68", "a69", "a70", "a71", "a72", "a73", "a74", "a75", "a76", "a77", "a78", "a79", "a80", "a81", "a82", "a83", "a84", "a85", "a86", "a87", "a88", "a89", "a90", "a91", "a92", "a93", "a94", "a95", "a96", "a97", "a98", "a99", "a100", "a101", "a102", "a103", "a104", "a105", "a106", "a107", "a108", "a109", "a110", "a111", "a112", "a113", "a114", "a115", "a116", "a117", "a118", "a119", "a120", "a121", "a122", "a123", "a124", "a125", "a126", "a127", "s40", "s41", "s42", "s43", "s44", "s45", "s46", "s47", "s48", "s49", "s50", "s51", "s52", "s53", "s54", "s55", "s56", "s57", "s58", "s59", "s60", "s61", "s62", "s63", "s64", "s65", "s66", "s67", "s68", "s69", "s70", "s71", "s72", "s73", "s74", "s75", "s76", "s77", "s78", "s79", "s80", "s81", "s82", "s83", "s84", "s85", "s86", "s87", "s88", "s89", "s90", "s91", "s92", "s93", "s94", "s95", "s96", "vcc", "scc", "memory"
typedef unsigned short u16;
typedef unsigned v4u __attribute__((ext_vector_type(4)));
typedef unsigned v2u __attribute__((ext_vector_type(2)));
typedef float f4 __attribute__((ext_vector_type(4)));
typedef __bf16 b2 __attribute__((ext_vector_type(2)));
typedef short s4 __attribute__((ext_vector_type(4)));
typedef __bf16 b8 __attribute__((ext_vector_type(8)));
typedef __amdgpu_buffer_rsrc_t rsrc_t;
#define LAS __attribute__((address_space(3)))
#define DI __device__ __forceinline__
constexpr int S = 2048, D = 512, F = 1376;
constexpr unsigned OFF_PX = 0x8000, OFF_PM = 0x18000, OFF_ST = OFF_PM + 4096;
constexpr unsigned OFF_U = 0x60000, SLOT = 0x20000, USLOT = 0x80000;
constexpr unsigned O_FLAG = 0x1E0000, F_K = O_FLAG, F_V = F_K + 32 * 128, F_G = F_V + 64 * 128, F_KM = F_G + 32 * 128, F_CNT = F_KM + 32 * 128,
                   F_TK = F_CNT + 64 * 512, F_ERR = F_TK + 128, F_X = F_ERR + 128, F_GO = F_X + 128, F_CK = F_GO + 2 * 16 * 128, F_XF = F_CK + 2 * 2048, F_HO = F_XF + 2 * 32 * 128, F_AR = F_HO + 32 * 128, F_L = F_AR + 32 * 128, F_G1 = F_L + 32 * 128, F_END = F_G1 + 32 * 128;
constexpr unsigned O_CAL = 0x1F8000, O_U = 2u << 20, O_XN = 18u << 20, O_PX = 20u << 20, O_PMX = 24u << 20, O_HO = 25u << 20, O_DBG = 26u << 20, O_KM = 236u << 20,
                   O_KX = 32u << 20, O_GP = 64u << 20, O_VOX = 108u << 20, O_ALL = 244u << 20;
constexpr int LDS_BYTES = 160 * 1024;
constexpr unsigned SPIN = 1u << 18;

DI float b2f(unsigned v) { return __uint_as_float(v << 16); }
DI float b2fh(unsigned v) { return __uint_as_float(v & 0xFFFF0000u); }
DI unsigned pk(float a, float b) { b2 r = {(__bf16)a, (__bf16)b}; return __builtin_bit_cast(unsigned, r); }
DI float rb(float f) { return b2f(pk(f, f) & 0xFFFFu); }
DI f4 mf32(v4u a, v4u b, f4 c) { return __builtin_amdgcn_mfma_f32_16x16x32_bf16(__builtin_bit_cast(b8, a), __builtin_bit_cast(b8, b), c, 0, 0, 0); }
typedef LAS unsigned char* lp;
DI lp mklp(unsigned a) { asm volatile("" : "+v"(a)); return (lp)(size_t)a; }
DI unsigned opq(unsigned a) { asm volatile("" : "+v"(a)); return a; }
DI int lane() { return opq(__builtin_amdgcn_mbcnt_hi(~0u, __builtin_amdgcn_mbcnt_lo(~0u, 0))); }
DI v2u trread(lp p) { return __builtin_bit_cast(v2u, __builtin_amdgcn_ds_read_tr16_b64_v4i16((LAS s4*)p)); }
DI v4u ldsr(lp p) { return *(LAS v4u*)p; }
DI void ldsw(lp p, v4u v) { *(LAS v4u*)p = v; }
#define LD16(r, vo, so) __builtin_amdgcn_raw_buffer_load_b128(r, vo, so, 16)
#define LD4(r, vo, so) __builtin_amdgcn_raw_buffer_load_b32(r, vo, so, 16)
#define ST16(d, r, vo, so) __builtin_amdgcn_raw_buffer_store_b128(d, r, vo, so, 16)
#define ST8(d, r, vo, so) __builtin_amdgcn_raw_buffer_store_b64(d, r, vo, so, 16)
#define ST4(d, r, vo, so) __builtin_amdgcn_raw_buffer_store_b32(d, r, vo, so, 16)
#define VMW(n) asm volatile("s_waitcnt vmcnt(" #n ")" ::: "memory")
#define BAR() asm volatile("s_waitcnt lgkmcnt(0)\n\ts_barrier" ::: "memory")
DI void dma(rsrc_t r, unsigned voff, unsigned soff, unsigned ldsdst) {
    unsigned keep;
    asm volatile("s_mov_b32 %0, m0\n\ts_mov_b32 m0, %4\n\ts_nop 0\n\tbuffer_load_dwordx4 %1, %2, %3 offen lds\n\ts_mov_b32 m0, %0"
                 : "=&s"(keep) : "v"(voff), "s"(r), "s"(soff), "s"(ldsdst) : "memory");
}
DI unsigned farx(unsigned base, unsigned L, unsigned pm) { const unsigned p = base + ((L >> 13) << 14) + (L & 8191); return p + ((~(pm ^ (p >> 21)) & 1) << 13); }
template <int C> DI float dppf(float v) { return __builtin_bit_cast(float, __builtin_amdgcn_update_dpp(0, __builtin_bit_cast(int, v), C, 0xf, 0xf, true)); }
DI rsrc_t mkr(const void* p, unsigned bytes) { return __builtin_amdgcn_make_buffer_rsrc((void*)p, 0, bytes, 0x00020000); }
DI bool wait_ge(rsrc_t rr, unsigned voff, unsigned need) {
#pragma unroll 1
    for (unsigned sp = 0; sp < SPIN; ++sp) {
        asm volatile("" ::: "memory");
        unsigned v = LD4(rr, voff, 0);
        if (__all(v >= need)) { asm volatile("" ::: "memory"); return true; }
        __builtin_amdgcn_s_sleep(4);
    }
    ST4(1u, rr, F_ERR, 0);
    return false;
}

#define RS(k)
#define FS(k)
#define FORCE_BAD(x) (x)
struct Args {
    const u16 *x, *mem0, *Wq, *Wk, *Wv, *Wo, *w1, *b1, *w2, *b2; u16* out; unsigned char* ws;
    int A, Per, RT, V, V2, pad, d1, d2, d3, d4, d5, d6;
};

typedef unsigned long long u64;
struct Tag { unsigned lo, hi, go, xf; };
DI void checkin(rsrc_t rr, const Tag& t, int att, unsigned payload) { const v2u r = {(t.lo & 0xFFFF0000u) | payload, t.hi}; ST8(r, rr, F_CK + att * 2048 + blockIdx.x * 8, 0);
}
DI unsigned gate(rsrc_t rr, const Tag& t, unsigned at) {
#pragma unroll 1
    for (unsigned sp = 0; sp < SPIN; ++sp) {
        asm volatile("" ::: "memory");
        const v2u v = __builtin_amdgcn_raw_buffer_load_b64(rr, 0, at, 16);
        const unsigned lo = __builtin_amdgcn_readfirstlane(v.x), hi = __builtin_amdgcn_readfirstlane(v.y);
        if ((((lo ^ t.lo) >> 16) | (hi ^ t.hi)) == 0) { asm volatile("" ::: "memory"); return lo & 3; }
        __builtin_amdgcn_s_sleep(4);
    }
    return ~0u;
}
DI bool xwait(rsrc_t rr, const Tag& t, int s0, int s1) {
    const int l = lane();
    const unsigned off = l == 0 ? t.go : t.xf + ((l & 1) ? s0 : s1) * 128;
#pragma unroll 1
    for (unsigned sp = 0; sp < SPIN; ++sp) {
        asm volatile("" ::: "memory");
        const v2u v = __builtin_amdgcn_raw_buffer_load_b64(rr, off, 0, 16);
        const u64 ok = __ballot((((v.x ^ t.lo) >> 16) | (v.y ^ t.hi)) == 0);
        if ((ok & 1) || (ok & 6) == 6) { asm volatile("" ::: "memory"); return true; }
        __builtin_amdgcn_s_sleep(4);
    }
    return false;
}
DI void master(rsrc_t rr, const Tag& t, int att, u64 xnew) {
    const int l = lane();
    v4u r0, r1;
#pragma unroll 1
    for (unsigned sp = 0;; ++sp) {
        asm volatile("" ::: "memory");
        r0 = LD16(rr, F_CK + att * 2048 + l * 32, 0); r1 = LD16(rr, F_CK + att * 2048 + l * 32 + 16, 0);
        const unsigned bad = (((r0.x ^ t.lo) | (r0.z ^ t.lo) | (r1.x ^ t.lo) | (r1.z ^ t.lo)) >> 16) | (r0.y ^ t.hi) | (r0.w ^ t.hi) | (r1.y ^ t.hi) | (r1.w ^ t.hi);
        if (__all(bad == 0)) break;
        if (sp >= SPIN) return;
        __builtin_amdgcn_s_sleep(4);
    }
    int vote = ((int)(r0.x << 16) >> 20) + ((int)(r0.z << 16) >> 20) + ((int)(r1.x << 16) >> 20) + ((int)(r1.z << 16) >> 20);
    unsigned m = (1u << (r0.x & 7)) | (1u << (r0.z & 7)) | (1u << (r1.x & 7)) | (1u << (r1.z & 7));
    m |= __shfl_xor(m, 1);
#pragma unroll
    for (int o = 32; o; o >>= 1) vote += __shfl_xor(vote, o);
    const unsigned pay = (vote > 0) | (__all(m == 255) ? 0 : 2);
    if (l < 16) { const v2u g = {(t.lo & 0xFFFF0000u) | pay, t.hi}; ST8(g, rr, F_GO + (att * 16 + l) * 128, 0); }
    if (l == 0) { const v2u x = {(unsigned)xnew, (unsigned)(xnew >> 32)}; ST8(x, rr, F_X, 0); }
}
DI float rsum(float v) {
    v += dppf<0x128>(v);
    { auto r = __builtin_amdgcn_permlane16_swap(__float_as_uint(v), __float_as_uint(v), false, false); v = __uint_as_float(r[0]) + __uint_as_float(r[1]); }
    { auto r = __builtin_amdgcn_permlane32_swap(__float_as_uint(v), __float_as_uint(v), false, false); v = __uint_as_float(r[0]) + __uint_as_float(r[1]); }
    return v;
}
template <class D0>
DI void init_ln(const Args& a, rsrc_t rr, int seg0, int w, D0 done0) {
    const int l = lane(), kh = (l >> 3) & 1, g = l >> 4;
    v4u xa[2][8];
    const u16* xp = a.x + (size_t)(seg0 * 32 + w * 8 + (l & 7)) * D + kh * 32 + g * 8;
#pragma unroll
    for (int i = 0; i < 16; ++i) xa[i >> 3][i & 7] = *(const v4u*)(xp + (i >> 3) * 1024 * D + (i & 7) * 64);
#pragma unroll
  for (int u = 0; u < 2; ++u) {
    const int seg = seg0 + 32 * u;
    const v4u (&xv)[8] = xa[u];
    if (w == 0 && l < 32) ST16((v4u{0, 0, 0, 0}), rr, F_CNT + seg * 512 + l * 16, 0);
    float s = 0;
#pragma unroll
    for (int i = 0; i < 8; ++i)
#pragma unroll
        for (int k = 0; k < 4; ++k) s += b2f(xv[i][k]) + b2fh(xv[i][k]);
    const float mean = rsum(s) * (1.f / D);
    float q = 0;
#pragma unroll
    for (int i = 0; i < 8; ++i)
#pragma unroll
        for (int k = 0; k < 4; ++k) { const float u = b2f(xv[i][k]) - mean, v = b2fh(xv[i][k]) - mean; q += u * u; q += v * v; }
    const float mb = rb(mean), inv = 1.f / rb(sqrtf(rb(rb(rsum(q) * (1.f / D)) + 1e-5f)));
    const unsigned vo = kh * 2048 + g * 256 + (l & 7) * 16;
#pragma unroll
    for (int i = 0; i < 8; ++i) {
        v4u o;
#pragma unroll
        for (int k = 0; k < 4; ++k) { const unsigned c = pk(b2f(xv[i][k]) - mb, b2fh(xv[i][k]) - mb); o[k] = pk(b2f(c) * inv, b2fh(c) * inv); }
        ST16(o, rr, vo, O_XN + seg * 32768 + i * 4096 + (w >> 1) * 1024 + (w & 1) * 128);
    }
    if (u == 0) done0();
  }
    VMW(0); BAR();
}
DI int probe(rsrc_t rr, unsigned off, unsigned delta, int kmin, unsigned ticks) {
    const int l = lane();
    const unsigned t0 = (unsigned)__builtin_amdgcn_s_memrealtime();
    unsigned m0 = ~0u >> 1, m1 = m0;
#pragma unroll 1
    for (int k = 0; k < 64; ++k) {
        const unsigned o = off + (k & 1) * delta;
        if (l < 8) ST16((v4u{0, 0, 0, 0}), rr, o + l * 16, 0);
        VMW(0);
        const unsigned c0 = (unsigned)__builtin_amdgcn_s_memtime();
        const unsigned v = __builtin_amdgcn_readfirstlane(LD4(rr, o, 0));
        asm volatile("" :: "s"(v) : "memory");
        const unsigned dt = (unsigned)__builtin_amdgcn_s_memtime() - c0;
        if (k & 1) { m1 = min(m1, dt); if (k >= kmin && (unsigned)__builtin_amdgcn_s_memrealtime() - t0 > ticks) break; } else m0 = min(m0, dt);
    }
    return (int)m0 - (int)m1;
}
DI void init_ring(const Args& a, rsrc_t rr, int h, int w) {
    const int l = lane();
    const v4u poison = {~0u, ~0u, ~0u, ~0u};
    {
        v4u m0 = poison;
        if (w == 2 && h < 4) {
            const u16* p = a.mem0 + (2 * ((l >> 2) & 3) + (l & 1)) * D + 256 * (h & 1) + 16 * (l >> 4) + 8 * ((l >> 1) & 1);
#pragma unroll
            for (int i = 0; i < 4; ++i) {
                v4u v = *(const v4u*)(p + 64 * i);
                v.x = min(v.x, ~1u); v.y = min(v.y, ~1u); v.z = min(v.z, ~1u); v.w = min(v.w, ~1u);
                ST16(v, rr, l * 16 + i * 1024, h * 4096);
            }
        }
        if (w == 1 || w == 2) {
#pragma unroll
            for (int i = 0; i < 8; ++i) if (w == 1 || (i < 4 && h >= 4)) ST16(m0, rr, l * 16 + (i & 3) * 1024, (w == 1 ? 1 + (i >> 2) : 0) * SLOT + h * 4096);
        } else if (w == 0 && l < 4) ST4(0u, rr, F_HO + l * 4096 + h * 128, 0);
        else if (w == 3 && l < 6) ST4(0u, rr, l == 0 ? F_K + h * 128 : l == 1 ? F_KM + h * 128 : l == 2 ? F_G + h * 128 : l < 5 ? F_V + h * 256 + (l - 3) * 128 : h ? F_ERR : F_TK, 0);
#pragma unroll
        for (int i = 0; i < 8; ++i) ST16(poison, rr, l * 16 + (i & 3) * 1024, (w >> 1) * USLOT + OFF_U + h * 16384 + (w & 1) * 8192 + (i >> 2) * 4096);
    }
}

DI void nap(int n) { for (int i = 0; i < n; ++i) __builtin_amdgcn_s_sleep(16); }
template <bool ISSUE, class SO, class PUT, class NX>
DI void load_slab(rsrc_t r, unsigned voff, unsigned wl, unsigned wr0, unsigned wr1, SO so, PUT put, bool two, NX next) {
    if (ISSUE) {
#pragma unroll
        for (int I = 0; I < 32; ++I) dma(r, voff, so(I), wl + I * 1024);
    }
    const lp p0 = mklp(wr0), p1 = mklp(wr1);
#pragma unroll
    for (int q = 0; q < 4; ++q) {
        if (two) VMW(24); else if (q == 0) VMW(24); else if (q == 1) VMW(16); else if (q == 2) VMW(8); else VMW(0);
#pragma unroll
        for (int I = 8 * q; I < 8 * q + 8; ++I) put(I, trread(p0 + I * 1024), trread(p1 + I * 1024));
        asm volatile("s_waitcnt lgkmcnt(0)" ::: "memory");
        __builtin_amdgcn_sched_barrier(0);
#pragma unroll
        for (int I = 8 * q; I < 8 * q + 8; ++I) next(I);
    }
}
DI void dma4(rsrc_t r, unsigned voff, unsigned soff, unsigned ldsdst) {
    unsigned keep;
    asm volatile("s_mov_b32 %0, m0\n\ts_mov_b32 m0, %4\n\ts_nop 0\n\tbuffer_load_dwordx4 %1, %2, %3 offen sc1 lds\n\tbuffer_load_dwordx4 %1, %2, %3 offen offset:1024 sc1 lds\n\t"
                 "buffer_load_dwordx4 %1, %2, %3 offen offset:2048 sc1 lds\n\tbuffer_load_dwordx4 %1, %2, %3 offen offset:3072 sc1 lds\n\ts_mov_b32 m0, %0"
                 : "=&s"(keep) : "v"(voff), "s"(r), "s"(soff), "s"(ldsdst) : "memory");
}
DI void stage_xn(rsrc_t rr, int seg, unsigned dst, int w, unsigned l16) {
    dma4(rr, l16, O_XN + seg * 32768 + w * 8192, dst + w * 8192);
    dma4(rr, l16, O_XN + seg * 32768 + w * 8192 + 4096, dst + w * 8192 + 4096);
}
#define SB() __builtin_amdgcn_sched_barrier(0)
template <class HK>
DI void proj(const v4u (&W1)[2][16], lp xb, f4 (&acc)[2][2], HK hook) {
    constexpr int PF = 3;
    v4u x[16][2];
#pragma unroll
    for (int t = 0; t < 2; ++t) { acc[t][0] = f4{0, 0, 0, 0}; acc[t][1] = f4{0, 0, 0, 0}; }
#pragma unroll
    for (int ks = 0; ks < PF; ++ks) { x[ks][0] = ldsr(xb + ks * 2048); x[ks][1] = ldsr(xb + ks * 2048 + 1024); }
    SB();
#pragma unroll
    for (int ks = 0; ks < 16; ++ks) {
        acc[0][0] = mf32(W1[0][ks], x[ks][0], acc[0][0]);
        if (ks + PF < 16) x[ks + PF][0] = ldsr(xb + (ks + PF) * 2048);
        SB();
        acc[1][0] = mf32(W1[1][ks], x[ks][0], acc[1][0]);
        hook(ks);
        SB();
        acc[0][1] = mf32(W1[0][ks], x[ks][1], acc[0][1]);
        if (ks + PF < 16) x[ks + PF][1] = ldsr(xb + (ks + PF) * 2048 + 1024);
        SB();
        acc[1][1] = mf32(W1[1][ks], x[ks][1], acc[1][1]);
        SB();
    }
}
template <int OFF> DI void dma1(rsrc_t r, unsigned voff, unsigned soff, unsigned ldsdst) {
    unsigned keep;
    asm volatile("s_mov_b32 %0, m0\n\ts_mov_b32 m0, %4\n\ts_nop 0\n\tbuffer_load_dwordx4 %1, %2, %3 offen offset:%5 sc1 lds\n\ts_mov_b32 m0, %0"
                 : "=&s"(keep) : "v"(voff), "s"(r), "s"(soff), "s"(ldsdst), "i"(OFF) : "memory");
}
DI void stage_piece(int i, rsrc_t rr, unsigned l16, unsigned so, unsigned dst) {
    const unsigned hi = (i >> 2) * 4096;
    switch (i & 3) { case 0: dma1<0>(rr, l16, so + hi, dst + hi); break; case 1: dma1<1024>(rr, l16, so + hi, dst + hi); break;
                     case 2: dma1<2048>(rr, l16, so + hi, dst + hi); break; default: dma1<3072>(rr, l16, so + hi, dst + hi); break; }
}
DI v4u chunk(f4 a, f4 b) { return v4u{pk(a[0], a[1]), pk(a[2], a[3]), pk(b[0], b[1]), pk(b[2], b[3])}; }

template <int KIND, class PRE>
DI unsigned producer(const Args& a, rsrc_t rr, const Tag& tg, int idx, int par, int w, PRE pre) {
    const int l = lane();
    const int g = l >> 4, c = l & 15;
    const unsigned l16 = opq(l * 16), wl = w * 32768;
    const unsigned wr0 = wl + (g * 4 + (c >> 2)) * 64 + (g & 1) * 32 + (c & 3) * 8, wr1 = wr0 ^ 32;
    const unsigned pc = ((l & 3) ^ (2 * (g & 1))) * 16, q4 = (l >> 2) & 3;
    constexpr unsigned RS1 = KIND == 3 ? F * 2 : 8192;
    v4u W1[2][16], W2[8][4];
    if (KIND == 3) nap(a.d5 & 255);
        float bias[8];
    constexpr int STEP = KIND == 0 ? 1 : 2;
    auto first = [&]() { const bool ok = xwait(rr, tg, par, par + STEP); if (ok) stage_xn(rr, par, 131072, w, l16); };
    {
        rsrc_t r1 = KIND == 0 ? mkr(a.Wk, D * 8192) : KIND == 2 ? mkr(a.Wv, D * 8192) : mkr(a.w1, D * F * 2);
        rsrc_t r2 = KIND == 2 ? mkr(a.Wo, 4096 * 1024) : mkr(a.w2, F * 1024);
        unsigned vo = (8 * g + q4) * RS1 + (idx * 128 + w * 32) * 2 + pc;
        if (KIND == 3 && idx * 128 + w * 32 >= F) vo = 0x80000000u;
        const unsigned vo2 = (idx * 128 + 4 * g + q4) * 1024 + w * 256 + pc;
        auto so2 = [](int I) { return (32 * ((I >> 1) & 3) + 16 * (I & 1)) * 1024 + (I >> 3) * 64; };
        auto so1 = [](int I) { return (32 * (I >> 1) + 4 * (I & 1)) * RS1; };
        auto put1 = [&](int I, v2u x, v2u y) { W1[0][I >> 1][2 * (I & 1)] = x.x; W1[0][I >> 1][2 * (I & 1) + 1] = x.y; W1[1][I >> 1][2 * (I & 1)] = y.x; W1[1][I >> 1][2 * (I & 1) + 1] = y.y; };
        auto put2 = [&](int I, v2u x, v2u y) { const int t = 2 * (I >> 3), k = (I >> 1) & 3, m = 2 * (I & 1); W2[t][k][m] = x.x; W2[t][k][m + 1] = x.y; W2[t + 1][k][m] = y.x; W2[t + 1][k][m + 1] = y.y; };
        if (KIND == 2 && par) {
            load_slab<true>(r2, vo2, wl, wr0, wr1, so2, put2, true, [&](int I) { dma(r1, vo, so1(I), wl + I * 1024); });
            load_slab<false>(r1, vo, wl, wr0, wr1, so1, put1, false, [](int) {});
            first();
        } else {
            load_slab<true>(r1, vo, wl, wr0, wr1, so1, put1, KIND >= 2, [&](int I) { if (KIND >= 2) dma(r2, vo2, so2(I), wl + I * 1024); });
            if (KIND >= 2) load_slab<false>(r2, vo2, wl, wr0, wr1, so2, put2, false, [](int) {});
            pre();
            first();
        }
        if (KIND == 3) {
#pragma unroll
            for (int i = 0; i < 8; ++i) { const int f = idx * 128 + w * 32 + (i >> 2) * 16 + 4 * g + (i & 3); bias[i] = f < F ? b2f(a.b1[f]) : 0.f; }
        }
    }
    BAR();
    constexpr int N = 64 / STEP, NI = KIND == 0 ? 4 : KIND == 2 ? 2 : 1;
    const unsigned flag = KIND == 0 ? F_K + idx * 128 : KIND == 2 ? F_V + idx * 256 + par * 128 : F_G + idx * 256 + par * 128;
    const lp exw = mklp(98304 + w * 1024 + l16), exr = mklp(98304 + l16);
    RS(0);
    unsigned pm = 0;
    stage_xn(rr, par + STEP, 0, w, l16);
    unsigned b0 = 131072, b1 = 0, b2 = 32768;
#pragma unroll 1
    for (int n = 0; n < N; ++n) {
        const int seg = par + n * STEP;
        if (n == 0) VMW(8); else if (KIND == 0) VMW(10); else VMW(16);
        BAR();
        if (n >= NI + 2 && w == 0 && l == 0) ST4((unsigned)(n - 1), rr, flag, 0);
        const unsigned sx = O_XN + min(seg + 2 * STEP, 63) * 32768 + w * 8192, dx = b2 + w * 8192;
        f4 acc[2][2];
        proj(W1, mklp(b0 + l16), acc, [&](int ks) { if (ks & 1) stage_piece(ks >> 1, rr, n ? l16 : 0xF0000000u, sx, dx); });
        if (n == 0) {
            pm = gate(rr, tg, tg.go);
            if (pm > 1) return pm;
            dma4(rr, l16, sx, dx); dma4(rr, l16, sx + 4096, dx + 4096);
        }
        if (KIND == 0) {
#pragma unroll
            for (int tt = 0; tt < 2; ++tt) ST16(chunk(acc[0][tt], acc[1][tt]), rr, l16 + tt * 4096, farx(O_KX, seg * 262144 + idx * 8192 + w * 1024, ~pm));
        } else {
            if (KIND == 3) {
#pragma unroll
                for (int t = 0; t < 2; ++t)
#pragma unroll
                    for (int tt = 0; tt < 2; ++tt)
#pragma unroll
                        for (int r = 0; r < 4; ++r) { const float h = rb(rb(acc[t][tt][r]) + bias[t * 4 + r]); const float p = h > 0.f ? h : 0.f; acc[t][tt][r] = p * p; }
            }
#pragma unroll
            for (int tt = 0; tt < 2; ++tt) ldsw(exw + tt * 4096, chunk(acc[0][tt], acc[1][tt]));
            BAR();
            v4u vf[2][4];
#pragma unroll
            for (int tt = 0; tt < 2; ++tt)
#pragma unroll
                for (int k = 0; k < 4; ++k) vf[tt][k] = ldsr(exr + tt * 4096 + k * 1024);
            f4 d[8][2];
            const unsigned so = KIND == 2 ? farx(O_VOX, seg * 1048576 + 8 * w * 32768 + idx * 1024, pm ^ (seg < NSEG)) : farx(O_GP, ((seg * 11 + idx) * 32 + 8 * w) * 1024, pm);
#pragma unroll
            for (int t = 0; t <= 8; ++t) {
                v4u o;
#pragma unroll
                for (int k = 0; k < 4; ++k) {
#pragma unroll
                    for (int tt = 0; tt < 2; ++tt) {
                        if (t < 8) { const f4 ci = k ? d[t][tt] : f4{0, 0, 0, 0}; d[t][tt] = KIND == 2 ? mf32(vf[tt][k], W2[t][k], ci) : mf32(W2[t][k], vf[tt][k], ci); }
                        if (t > 0 && k >= 1 && k <= 2) o[2 * (k - 1) + tt] = pk(d[t - 1][k - 1][2 * tt], d[t - 1][k - 1][2 * tt + 1]);
                        if (t > 0 && k == 3 && tt == 0) ST16(o, rr, l16, so + (t - 1) * (KIND == 2 ? 65536 : 1024));
                        SB();
                    }
                }
            }
        }
        if (n < NI) {
            if (n == 0) RS(1);
            VMW(0); BAR();
            if (w == 0 && l == 0) ST4((unsigned)(n + 1), rr, flag, 0);
            if (n == (KIND == 0 ? 2 : 0)) RS(2);
        }
        const unsigned tb = b0; b0 = b1; b1 = b2; b2 = tb;
    }
    VMW(0); BAR();
    if (w == 0 && l == 0) ST4((unsigned)N, rr, flag, 0);
    return pm;
}

DI float xmax(float v) {
    { auto r = __builtin_amdgcn_permlane16_swap(__float_as_uint(v), __float_as_uint(v), false, false); v = fmaxf(__uint_as_float(r[0]), __uint_as_float(r[1])); }
    { auto r = __builtin_amdgcn_permlane32_swap(__float_as_uint(v), __float_as_uint(v), false, false); v = fmaxf(__uint_as_float(r[0]), __uint_as_float(r[1])); }
    return v;
}
DI float xsum(float v) {
    { auto r = __builtin_amdgcn_permlane16_swap(__float_as_uint(v), __float_as_uint(v), false, false); v = __uint_as_float(r[0]) + __uint_as_float(r[1]); }
    { auto r = __builtin_amdgcn_permlane32_swap(__float_as_uint(v), __float_as_uint(v), false, false); v = __uint_as_float(r[0]) + __uint_as_float(r[1]); }
    return v;
}
DI float soft(f4& ta, f4& tb, f4& tm, bool memlane) {
    float mx = -INFINITY;
#pragma unroll
    for (int r = 0; r < 4; ++r) {
        tm[r] = memlane ? rb(rb(tm[r]) * 0.08838834764831845f) : -INFINITY;
        mx = fmaxf(mx, fmaxf(fmaxf(ta[r], tb[r]), tm[r]));
    }
    mx = xmax(mx);
    float sum = 0;
#pragma unroll
    for (int r = 0; r < 4; ++r) {
        ta[r] = __builtin_amdgcn_exp2f((ta[r] - mx) * 1.4426950408889634f); tb[r] = __builtin_amdgcn_exp2f((tb[r] - mx) * 1.4426950408889634f);
        tm[r] = __builtin_amdgcn_exp2f((tm[r] - mx) * 1.4426950408889634f);
        sum += (ta[r] + tb[r]) + tm[r];
    }
    return __builtin_amdgcn_rcpf(xsum(sum));
}
constexpr int QN0 = 16;
DI void qworker(const Args& a, rsrc_t rr, unsigned pm, int h, int w, int first) {
    const int l = lane();
    const int g = l >> 4, c = l & 15;
    const unsigned l16 = opq(l * 16), wl = w * 32768;
    const unsigned wr0 = wl + (g * 4 + (c >> 2)) * 64 + (g & 1) * 32 + (c & 3) * 8, wr1 = wr0 ^ 32;
    const unsigned pc = ((l & 3) ^ (2 * (g & 1))) * 16, q4 = (l >> 2) & 3;
    v4u W1[2][16];
    if (first == 0) nap(a.d5 & 255);
    load_slab<true>(mkr(a.Wq, D * 8192), (8 * g + q4) * 8192 + (h * 128 + w * 32) * 2 + pc, wl, wr0, wr1, [](int I) { return (32 * (I >> 1) + 4 * (I & 1)) * 8192u; },
              [&](int I, v2u x, v2u y) { W1[0][I >> 1][2 * (I & 1)] = x.x; W1[0][I >> 1][2 * (I & 1) + 1] = x.y; W1[1][I >> 1][2 * (I & 1)] = y.x; W1[1][I >> 1][2 * (I & 1) + 1] = y.y; }, false, [](int) {});
    BAR();
    const lp exw = mklp(65536 + w * 1024 + l16), exr = mklp(65536 + w * 4096 + l16);
    const int n0 = first ? 31 : 0;
    stage_xn(rr, 2 * n0, 0, w, l16); stage_xn(rr, 2 * n0 + 1, 32768, w, l16);
    bool ho = first != 0;
    const int tt = w & 1;
    const unsigned kmo = g * 128 + (2 * ((c >> 2) & 1) + (c & 1) + 4 * ((c >> 1) & 1)) * 16;
    unsigned b0 = 0, b1 = 32768, b2 = 81920, kseen = 0, pend = 0;
    v4u q[4] = {};
    f4 ta = {0, 0, 0, 0}, tb = ta;
#pragma unroll 1
    for (int n = n0, np = -1;;) {
        const int nn = ho ? first : n + (n < QN0 ? 1 : 2), segn = 2 * n + (w >> 1), seg = 2 * np + (w >> 1);
        v4u ka[4], kb[4], km[4];
        bool early = false;
        if (n < 32) {
            if (kseen <= (unsigned)segn) { if (!wait_ge(rr, F_K + h * 128, segn + 1)) return; kseen = __builtin_amdgcn_readfirstlane(LD4(rr, F_K + h * 128, 0)); }
            if (pend) VMW(2); else VMW(0);
            BAR();
            unsigned fkm;
            asm volatile("buffer_load_dword %0, off, %1, %2 sc1" : "=v"(fkm) : "s"(rr), "s"(F_KM + h * 128) : "memory");
            f4 acc[2][2];
            {
                const unsigned sx = O_XN + min(2 * nn, 63) * 32768 + w * 8192, dx = b2 + w * 8192;
                proj(W1, mklp(b0 + l16), acc, [&](int ks) { if (ks & 1) stage_piece(ks >> 1, rr, l16, sx, dx); });
            }
#pragma unroll
            for (int t2 = 0; t2 < 2; ++t2) ldsw(exw + t2 * 4096, chunk(acc[0][t2], acc[1][t2]));
            asm volatile("s_waitcnt vmcnt(8)" : "+v"(fkm) :: "memory");
            if (pend) { if (l == 0) __builtin_amdgcn_raw_ptr_buffer_atomic_add_i32(1, rr, pend, 0, 16); pend = 0; }
            early = np >= 0 && __builtin_amdgcn_readfirstlane(fkm) > (unsigned)seg;
            if (early) {
                asm volatile("" ::: "memory");
#pragma unroll
                for (int k = 0; k < 4; ++k) km[k] = LD16(rr, kmo + k * 512, farx(O_KM, seg * 65536 + h * 2048, ~pm));
            }
#pragma unroll
            for (int k = 0; k < 4; ++k) { const unsigned ko = farx(O_KX, segn * 262144 + h * 8192, ~pm); ka[k] = LD16(rr, l16 + k * 1024, ko); kb[k] = LD16(rr, l16 + k * 1024, ko + 4096); }
            BAR();
            {
                const unsigned sx = O_XN + min(2 * nn + 1, 63) * 32768 + w * 8192, dx = b0 + w * 8192;
                proj(W1, mklp(b1 + l16), acc, [&](int ks) { if (ks & 1) stage_piece(ks >> 1, rr, l16, sx, dx); });
            }
#pragma unroll
            for (int t2 = 0; t2 < 2; ++t2) ldsw(exw + (2 + t2) * 4096, chunk(acc[0][t2], acc[1][t2]));
            BAR();
            { const unsigned t0 = b0, t1 = b1; b0 = b2; b1 = t0; b2 = t1; }
        }
        if (np >= 0) {
            if (!early) {
                if (!wait_ge(rr, F_KM + h * 128, seg + 1)) return;
#pragma unroll
                for (int k = 0; k < 4; ++k) km[k] = LD16(rr, kmo + k * 512, farx(O_KM, seg * 65536 + h * 2048, ~pm));
            }
            f4 tm = {0, 0, 0, 0};
#pragma unroll
            for (int k = 0; k < 4; ++k) tm = mf32(km[k], q[k], tm);
            const float inv = soft(ta, tb, tm, g < 2);
            ST16(chunk(ta * inv, tb * inv), rr, l16, O_PX + ((seg * 2 + tt) * 32 + h) * 1024);
            if (g < 2) { v2u pv = {pk(tm[0] * inv, tm[1] * inv), pk(tm[2] * inv, tm[3] * inv)}; ST8(pv, rr, c * 16 + g * 8, O_PMX + (seg * 2 + tt) * 8192 + h * 256); }
            pend = F_CNT + seg * 512 + (h & 3) * 128;
            if (!early || n >= 30) { VMW(0); if (l == 0) __builtin_amdgcn_raw_ptr_buffer_atomic_add_i32(1, rr, pend, 0, 16); pend = 0; }
        }
        if (n < 32) {
#pragma unroll
            for (int k = 0; k < 4; ++k) q[k] = ldsr(exr + k * 1024);
            ta = f4{0, 0, 0, 0}; tb = ta;
#pragma unroll
            for (int k = 0; k < 4; ++k) { ta = mf32(ka[k], q[k], ta); tb = mf32(kb[k], q[k], tb); }
#pragma unroll
            for (int r = 0; r < 4; ++r) {
                const int key = 4 * g + r, qi = 16 * tt + c;
                ta[r] = key <= qi ? rb(rb(ta[r]) * 0.08838834764831845f) : -1e9f;
                tb[r] = key + 16 <= qi ? rb(rb(tb[r]) * 0.08838834764831845f) : -1e9f;
            }
            if (segn == 63 && !ho) break;
            if (segn == 63) {
#pragma unroll
                for (int k = 0; k < 4; ++k) ST16(q[k], rr, l16 + k * 1024, O_HO + (h * 2 + tt) * 6144);
                ST16(__builtin_bit_cast(v4u, ta), rr, l16, O_HO + (h * 2 + tt) * 6144 + 4096); ST16(__builtin_bit_cast(v4u, tb), rr, l16 + 1024, O_HO + (h * 2 + tt) * 6144 + 4096);
                ST16((v4u{~0u, ~0u, ~0u, ~0u}), rr, l16, O_PX + ((126 + tt) * 32 + h) * 1024);
                if (l < 16) ST16((v4u{~0u, ~0u, ~0u, ~0u}), rr, l16, O_PMX + (126 + tt) * 8192 + h * 256);
                VMW(0);
                if (l == 0) __builtin_amdgcn_raw_ptr_buffer_atomic_add_i32(1, rr, F_HO + h * 128, 0, 16);
            }
        } else break;
        np = ho ? -1 : n; n = nn; ho = false;
    }
}

DI bool wait_wg(rsrc_t rr, unsigned voff, unsigned need, int w, LAS unsigned* okw) {
    if (w == 0) { const bool ok = wait_ge(rr, voff, need); *okw = ok; }
    BAR();
    const bool ok = __builtin_amdgcn_readfirstlane(*okw);
    asm volatile("" ::: "memory");
    return ok;
}
DI float gate(float s, float b) { const float o = rb(rb(s) + b); return rb(o * __builtin_amdgcn_rcpf(1.f + __builtin_amdgcn_exp2f(-o * 1.4426950408889634f))); }
constexpr unsigned NCO = 61 * 8;
template <int NJ>
DI bool fine(const Args& a, rsrc_t rr, unsigned pm, int w, int l, int seg, int tt, int j0, LAS unsigned* tk) {
    const int g = l >> 4, c = l & 15, r0 = (j0 / NJ) & 7, j = j0 + (w & (NJ - 1));
    const unsigned l16 = opq(l * 16);
    if (!wait_wg(rr, (l < 32 ? F_V + l * 256 : F_G + min(l - 32, 10) * 256) + (seg & 1) * 128, (seg >> 1) + 1, w, tk + 1)) return false;
    FS(0);
    v4u A[NJ][10], B[10];
    v2u G[11];
#pragma unroll
    for (int u = 0; u < NJ; ++u)
#pragma unroll
        for (int i = 0; i < 8; ++i) A[u][i] = LD16(rr, l16, farx(O_VOX, seg * 1048576 + (j0 + u) * 32768 + (8 * w + ((i + r0) & 7)) * 1024, pm));
#pragma unroll
    for (int i = 0; i < 11; ++i) G[i] = __builtin_amdgcn_raw_buffer_load_b64(rr, l16 + tt * 8, farx(O_GP, ((seg * 11 + i) * 32 + j) * 1024, pm), 16);
    const v2u xv = *(const v2u*)(a.x + (size_t)(seg * 32 + tt * 16 + c) * D + 16 * j + 4 * g);
    const v2u bb = *(const v2u*)(a.b2 + 16 * j + 4 * g);
    SB();
    float gt[4];
#pragma unroll
    for (int e = 0; e < 4; ++e) {
        float s = 0;
#pragma unroll
        for (int i = 0; i < 11; ++i) s += (e & 1) ? b2fh(G[i][e >> 1]) : b2f(G[i][e >> 1] & 0xFFFF);
        const unsigned bw = (e & 2) ? bb.y : bb.x;
        gt[e] = gate(s, (e & 1) ? b2fh(bw) : b2f(bw & 0xFFFF));
    }
    FS(1);
    const lp pw = mklp(w * 1024 + l16), pr = mklp((w & (NJ - 1)) * 4096 + l16);
    const rsrc_t ro = mkr(a.out, S * D * 2);
#pragma unroll 1
    for (unsigned sp = 0;; ++sp) {
        const unsigned vo = sp ? l16 : 0xF0000000u;
        asm volatile("" ::: "memory");
#pragma unroll
        for (int i = 0; i < 8; ++i) B[i] = LD16(rr, vo, O_PX + ((seg * 2 + tt) * 32 + 8 * w + ((i + r0) & 7)) * 1024);
#pragma unroll
        for (int i = 0; i < 2; ++i) B[8 + i] = LD16(rr, vo + i * 1024, O_PMX + (seg * 2 + tt) * 8192 + w * 2048);
#pragma unroll
        for (int u = 0; u < NJ; ++u)
#pragma unroll
            for (int i = 0; i < 2; ++i) A[u][8 + i] = LD16(rr, vo + i * 1024, (seg % 3) * USLOT + OFF_U + pm * 8192 + (j0 + u) * 16384 + w * 2048);
        SB();
        unsigned m = 0;
        if (NJ == 2) {
#pragma unroll
            for (int i = 0; i < 14; ++i) { const v4u v = i < 10 ? B[i] : A[(i - 10) >> 1][8 + (i & 1)]; m = max(max(m, max(v.x, v.y)), max(v.z, v.w)); }
        }
#pragma unroll
        for (int u = 0; u < NJ; ++u) {
            f4 d = {0, 0, 0, 0};
#pragma unroll
            for (int i = 0; i < 10; ++i) d = mf32(A[u][i], B[i], d);
            ldsw(pw + u * 4096, __builtin_bit_cast(v4u, d));
        }
        if (NJ == 2 && seg == 63 && !__all(m != ~0u)) {
            if (sp < SPIN) { __builtin_amdgcn_s_sleep(2); continue; }
            ST4(1u, rr, F_ERR, 0);
        }
        BAR();
        if (sp) FS(3);
        if (w < NJ) {
            f4 d = __builtin_bit_cast(f4, ldsr(pr));
#pragma unroll
            for (int i = 1; i < 4; ++i) d += __builtin_bit_cast(f4, ldsr(pr + i * 1024));
            v2u o;
            o.x = pk(b2f(xv.x & 0xFFFF) + rb(rb(d[0]) * gt[0]), b2fh(xv.x) + rb(rb(d[1]) * gt[1]));
            o.y = pk(b2f(xv.y & 0xFFFF) + rb(rb(d[2]) * gt[2]), b2fh(xv.y) + rb(rb(d[3]) * gt[3]));
            __builtin_amdgcn_raw_buffer_store_b64(o, ro, sp ? ((seg * 32 + tt * 16 + c) * D + 16 * j + 4 * g) * 2 : 0xF0000000u, 0, 0);
        }
        if (sp) break;
        if (!wait_wg(rr, seg == 63 ? F_L + (j0 + tt) * 128 : l == 4 ? F_KM + (j0 + tt + 2 * (seg & 1)) * 128 : F_CNT + seg * 512 + (l & 3) * 128, (l == 4 || seg == 63) ? min(seg + 2, 64) : 16, w, tk + 2)) return false;
        FS(2);
    }
    return true;
}
DI int finisher(const Args& a, rsrc_t rr, unsigned pm, int w) {
    const int l = lane();
    const int g = l >> 4, c = l & 15;
    const unsigned l16 = opq(l * 16);
    LAS unsigned* tk = (LAS unsigned*)(size_t)(81920);
    const lp pw = mklp(w * 16384 + l16), pw2 = mklp(65536 + w * 4096 + l16), pr = mklp(l16), pr2 = mklp(65536 + l16);
    unsigned nxt = 0;
    if (w == 0 && l == 0) nxt = __builtin_amdgcn_raw_ptr_buffer_atomic_add_i32(1, rr, F_TK, 0, 16);
    for (;;) {
        BAR();
        if (w == 0 && l == 0) *tk = nxt;
        BAR();
        const unsigned item = __builtin_amdgcn_readfirstlane(*tk);
        if (item >= NCO) {
            const int f = item - NCO;
            if (f < 32) fine<4>(a, rr, pm, w, l, 61 + (f >> 4), f & 1, ((f >> 1) & 7) * 4, tk);
            else if (f < 64) { fine<2>(a, rr, pm, w, l, 63, f & 1, f & 30, tk); asm volatile(".ifndef km_mark\nkm_mark:\n.endif"); }
            return -1;
        }
        const int seg = item >> 3, j = (item & 7) * 4 + w;
        if (!wait_wg(rr, (l < 32 ? F_V + l * 256 : F_G + min(l - 32, 10) * 256) + (seg & 1) * 128, (seg >> 1) + 1, w, tk + 1)) return -1;
        v4u A[40], G[11];
#pragma unroll
        for (int i = 0; i < 32; ++i) A[i] = LD16(rr, l16 + (i & 3) * 1024, farx(O_VOX, seg * 1048576 + j * 32768, pm ^ (seg < NSEG)) + (i >> 3) * 16384 + ((i >> 2) & 1) * 4096);
#pragma unroll
        for (int i = 0; i < 11; ++i) G[i] = LD16(rr, l16, farx(O_GP, ((seg * 11 + i) * 32 + j) * 1024, pm));
        v2u xv[2];
#pragma unroll
        for (int tt = 0; tt < 2; ++tt) xv[tt] = *(const v2u*)(a.x + (size_t)(seg * 32 + tt * 16 + c) * D + 16 * j + 4 * g);
        const v2u bb = *(const v2u*)(a.b2 + 16 * j + 4 * g);
        SB();
        float gt[8];
#pragma unroll
        for (int e = 0; e < 8; ++e) {
            float s = 0;
#pragma unroll
            for (int i = 0; i < 11; ++i) s += (e & 1) ? b2fh(G[i][e >> 1]) : b2f(G[i][e >> 1] & 0xFFFF);
            const unsigned bw = (e & 2) ? bb.y : bb.x;
            gt[e] = gate(s, (e & 1) ? b2fh(bw) : b2f(bw & 0xFFFF));
        }
        if (!wait_wg(rr, F_CNT + seg * 512 + (l & 3) * 128, 16, w, tk + 2)) return -1;
        {
            v4u p[20];
#pragma unroll
            for (int i = 0; i < 16; ++i) p[i] = LD16(rr, l16 + (i & 3) * 1024, O_PX + seg * 65536 + w * 16384 + (i >> 2) * 4096);
#pragma unroll
            for (int i = 0; i < 4; ++i) p[16 + i] = LD16(rr, l16 + i * 1024, O_PMX + seg * 16384 + w * 4096);
            SB();
#pragma unroll
            for (int i = 0; i < 16; ++i) ldsw(pw + i * 1024, p[i]);
#pragma unroll
            for (int i = 0; i < 4; ++i) ldsw(pw2 + i * 1024, p[16 + i]);
        }
        if (!wait_wg(rr, seg < 57 ? F_KM + (item & 31) * 128 : F_AR + ((item & 7) * 4 + (l & 3)) * 128, seg < 57 ? seg + 4 : seg + 1, w, tk + 3)) return -1;
#pragma unroll
        for (int i = 0; i < 8; ++i) A[32 + i] = LD16(rr, l16 + (i & 3) * 1024, O_U + seg * 262144 + j * 8192 + (i >> 2) * 4096);
        if (w == 0 && l == 0) nxt = __builtin_amdgcn_raw_ptr_buffer_atomic_add_i32(1, rr, F_TK, 0, 16);
        f4 d[2] = {{0, 0, 0, 0}, {0, 0, 0, 0}};
        {
            constexpr int PF = 5;
            v4u y[40][2];
            auto rd = [&](int i, int tt) { return ldsr(i < 32 ? pr + tt * 32768 + i * 1024 : pr2 + tt * 8192 + (i - 32) * 1024); };
#pragma unroll
            for (int i = 0; i < PF; ++i) { y[i][0] = rd(i, 0); y[i][1] = rd(i, 1); }
            SB();
#pragma unroll
            for (int i = 0; i < 40; ++i) {
                d[0] = mf32(A[i], y[i][0], d[0]);
                if (i + PF < 40) y[i + PF][0] = rd(i + PF, 0);
                SB();
                d[1] = mf32(A[i], y[i][1], d[1]);
                if (i + PF < 40) y[i + PF][1] = rd(i + PF, 1);
                SB();
            }
        }
#pragma unroll
        for (int tt = 0; tt < 2; ++tt) {
            v2u o;
            o.x = pk(b2f(xv[tt].x & 0xFFFF) + rb(rb(d[tt][0]) * gt[4 * tt]), b2fh(xv[tt].x) + rb(rb(d[tt][1]) * gt[4 * tt + 1]));
            o.y = pk(b2f(xv[tt].y & 0xFFFF) + rb(rb(d[tt][2]) * gt[4 * tt + 2]), b2fh(xv[tt].y) + rb(rb(d[tt][3]) * gt[4 * tt + 3]));
            __builtin_amdgcn_raw_buffer_store_b64(o, mkr(a.out, S * D * 2), ((seg * 32 + tt * 16 + c) * D + 16 * j + 4 * g) * 2, 0, 0);
        }
    }
}

template <int att>
DI bool run(const Args& a, rsrc_t rr, u64& X, v2u xr, unsigned xcc, int w, unsigned t00) {
    const int b = blockIdx.x, br = b >> 3;

    const int bx = att ? b & 7 : FORCE_BAD(xcc);
    int vote = 0;
    Tag tg;
    auto mktag = [&]() {
        if (att == 0) X = (unsigned)__builtin_amdgcn_readfirstlane(xr.x) | ((u64)(unsigned)__builtin_amdgcn_readfirstlane(xr.y) << 32);
        const u64 E = X + (att + 1) * 0x9E3779B97F4B0000ull;
        tg = Tag{(unsigned)E, (unsigned)(E >> 32), F_GO + (att * 16 + (b & 15)) * 128, F_XF + att * 4096};
    };
    if (bx > 3 && br >= 16) {
        const unsigned sg = (br & 7) * 4 + bx - 4;
        const int l = lane();
        auto pullq = [&](int i0, int i1) {
#pragma unroll
            for (int i = i0; i < i1; ++i) {
                unsigned keep;
                asm volatile("s_mov_b32 %0, m0\n\ts_mov_b32 m0, %4\n\ts_nop 0\n\tbuffer_load_dword %1, %2, %3 offen lds\n\ts_mov_b32 m0, %0"
                             : "=&s"(keep) : "v"((w * 32 + (l >> 1)) * 8192 + sg * 256 + (l & 1) * 128), "s"(mkr(a.Wq, D * 8192)), "s"(i * 128 * 8192), "s"(w * 32768 + 256) : "memory");
            }
        };
        if (br >= 24) { init_ring(a, rr, sg, w); mktag(); pullq(1, 4); VMW(3); }
        else init_ln(a, rr, sg, w, [&]() { if (br == 16) { VMW(0); BAR(); mktag(); if (w == 0 && l == 0) { const v2u r = {tg.lo, tg.hi}; ST8(r, rr, tg.xf + sg * 128, 0); } } });
    }
    if (bx < 3 && br >= 11 && br < 20) if (w == 0) { vote = max(-300, min(300, probe(rr, O_CAL + (bx * 9 + br - 11) * 256, 8192, 3, a.V2 >> 8))); if (xcc >> 2) vote = -vote; }
    mktag();
    asm volatile("s_barrier" : "+s"(tg.lo) :: "memory");
    if (w == 0 && lane() == 0) checkin(rr, tg, att, ((vote & 0xFFF) << 4) | bx);
    const bool ms = (b >> 1) == 64 && w == 3;
    const u64 xnew = X + 2 * 0x9E3779B97F4B0000ull;
    unsigned pm = 0;
    if (bx < 3) {
        const int nv = bx < 2 ? 11 : 10;
        const int role = br < nv ? 1 : br < 2 * nv ? 2 : 0, h = role == 0 ? bx * 10 + br - 2 * nv : bx * 11 + br - (role - 1) * nv;
        const int l = lane(), g = l >> 4, c = l & 15;
        const unsigned sidx = (l & 2) ? 0xEEEEu : 0x4444u;
        auto cl = [](int half, int i) { return 4 * (i >> 1) + 2 * half + (i & 1); };
        const unsigned l16 = l * 16, l16b = l * 16 + 4096;
        const unsigned sto = OFF_ST + l * 16;
        const unsigned st = 98304 + w * 2048;
        const unsigned long long mx = 0x00FF00FF00FF00FFull;
        int fail = 0;
#define COMMON [odd] "s"((bx & 1) * 2), [e1] "s"(a.d6), [A] "s"(a.A), [g1] "s"(F_G1 + ((b + w * 4) & 15) * 128), [XM] "s"(a.d2 & 255), [V] "s"(a.V), [RT] "s"(a.RT), [V2] "s"(a.V2 & 255), [per] "s"(a.Per), [sto] "v"(sto), [st] "s"(st), [l16] "v"(l16), [l16b] "v"(l16b), [rr] "s"(rr), [sidx] "v"(sidx)
        if (role == 2) {
            const unsigned mo = h * 256 + cl(c >> 3, 2 * g + (c & 1)) * 16 + (c & 6) * 2;
            const unsigned mlo = ((2 * g + (c & 1)) * D + 16 * h + (c & 14)) * 2;
            const unsigned sel = (c & 1) ? 0x03020706u : 0x05040100u;
            if ((b >> 1) == 64) { if (ms) master(rr, tg, att, xnew); }
            pm = gate(rr, tg, tg.go);
            if (pm == 2 || pm == 3) { BAR(); return true; }
            if (pm > 1) return false;
            unsigned cls = 1;
            if (h == 5 && w == 2) {
                cls = 1 + 2 * (probe(rr, OFF_PM + 6144 + pm * 8192, 16384, 7, 300) > 0);
            }
            asm volatile(ASM_OW : [fail] "=s"(fail)
                : COMMON, [pol] "s"(pm), [vox] "s"(O_VOX + h * 65536 + (~(pm ^ (NSEG > 0)) & 1) * 8192), [ml] "s"(a.ws + O_DBG + (1u << 20)), [uoff] "s"(OFF_U + h * 16384), [w] "s"(w), [ua] "s"(O_U + h * 8192), [far] "s"(F_AR + h * 128), [dv] "s"(a.d1), [dv3] "s"(w == 1 ? a.d3 & 255 : a.d3 >> 8), [sel] "v"(sel), [mx] "s"(mx), [mo] "v"(mo), [og] "s"(a.d2 >> 8), [cls] "s"(__builtin_amdgcn_readfirstlane(cls)), [bc] "s"(h == 5 ? a.pad : 0), [g1w] "v"(F_G1 + l * 128), [mo2] "v"(mo + 0x4000), [mz] "v"(h * 256 + c * 16 + (g & 1) * 0x4000),
                  [stw] "v"(OFF_ST + h * 16), [mlo] "v"(mlo), [fv] "v"(F_V + ((w >> 1) * 16 + c) * 256) : CLOB_OW);
        } else {
            const unsigned wl = w * 32768, q4 = (l >> 2) & 3, pc = ((l & 3) ^ (2 * (g & 1))) * 16;
            const unsigned wr0 = wl + (g * 4 + (c >> 2)) * 64 + (g & 1) * 32 + (c & 3) * 8, wr1 = wr0 ^ 32;
            const unsigned wva = (16 * (g >> 1) + 8 * (q4 >> 1) + 4 * (g & 1) + (q4 & 1)) * 8192 + (h * 128 + w * 32) * 2 + pc;
            if (role == 0) {
                const unsigned pxo = OFF_PX + (h >> 4) * 0x8000 + (h & 15) * 512 + (g >> 1) * 256 + cl(g & 1, l & 7) * 16 + (c >> 3) * 0x4000;
                const unsigned pmo = OFF_PM + (h >> 3) * 1024 + (((h >> 1) & 3) * 16 + cl(g & 1, l & 7)) * 16 + (h & 1) * 8 + (c >> 3) * 0x4000;
                const float ninf = g < 2 ? -INFINITY : 0.f;
                const unsigned ldw = (32 * w + c) * 32 + 4 * g, ldr = 8 * l;
                const unsigned kxo = ((c >> 1) & 1) * 4096 + g * 256 + (8 * (c >> 3) + 2 * ((c >> 2) & 1) + (c & 1)) * 16;
                asm volatile(ASM_QK : [fail] "=s"(fail)
                    : COMMON, [wa] "s"(mkr(a.Wq, D * 8192)), [wb] "s"(mkr(a.Wk, D * 8192)), [wva] "v"(wva), [wl] "s"(wl), [go] "s"(tg.go), [tlo] "s"(tg.lo), [thi] "s"(tg.hi), [wr0] "v"(wr0), [wr1] "v"(wr1), [w] "s"(w),
                      [kx] "s"(O_KX + h * 16384), [km] "s"(O_KM + (h >> 2) * 16384 + (h & 3) * 2048), [kxo] "v"(kxo), [psw] "s"(OFF_ST + 512 + h * 16), [fl] "s"(F_L + h * 128), [ho] "s"(O_HO + h * 12288), [fho] "v"(F_HO + h * 128), [kmo] "v"(g * 128 + (l & 7) * 16), [fk] "v"(F_K + h * 128), [fkm] "v"(F_KM + h * 128),
                      [mx] "s"(mx), [pxo] "v"(pxo), [pmo] "v"(pmo), [ninf] "v"(ninf), [ldw] "v"(ldw), [ldr] "v"(ldr) : CLOB_W);
                if (fail == 0 && w < 2) {
                    const int l2 = lane(), g2 = l2 >> 4;
                    const lp im = mklp(4096 + 8 * l2), hq = mklp(0xA000 + w * 6144 + 16 * l2);
                    f4 tm = {0, 0, 0, 0};
#pragma unroll
                    for (int k = 0; k < 4; ++k) { const v2u lo = trread(im + k * 1024), hi = trread(im + k * 1024 + 512); tm = mf32(v4u{lo.x, lo.y, hi.x, hi.y}, ldsr(hq + k * 1024), tm); }
                    f4 ta = __builtin_bit_cast(f4, ldsr(hq + 4096)), tb = __builtin_bit_cast(f4, ldsr(hq + 5120));
                    const float inv = soft(ta, tb, tm, g2 >= 2);
                    ST16(chunk(ta * inv, tb * inv) & 0x7FFF7FFFu, rr, 16 * l2, O_PX + ((126 + w) * 32 + h) * 1024);
                    if (g2 >= 2) {
                        ST4(pk(tm[0] * inv, tm[1] * inv) & 0x7FFF7FFFu, rr, (l2 & 15) * 16 + (g2 & 1) * 4, O_PMX + (126 + w) * 8192 + h * 256);
                        ST4(pk(tm[2] * inv, tm[3] * inv) & 0x7FFF7FFFu, rr, (l2 & 15) * 16 + (g2 & 1) * 4 + 8, O_PMX + (126 + w) * 8192 + h * 256);
                    }
                }
            } else {
                const unsigned wvb = (h * 128 + 32 * (g & 1) + 16 * (q4 >> 1) + 4 * (g >> 1) + (q4 & 1)) * 1024 + w * 256 + pc;
                const unsigned pf0 = l < 16 ? O_VOX + h * 65536 + 32768 + (w * 16 + l) * 128 : 0xF0000000u;
                const unsigned uo = OFF_U + (8 * w + g) * 16384 + h * 256 + c * 16;
                const unsigned ldw = (16 * w + c) * 32 + 8 * g, ldr = 8 * l;
                asm volatile(ASM_VO : [fail] "=s"(fail)
                    : COMMON, [wa] "s"(mkr(a.Wv, D * 8192)), [wb] "s"(mkr(a.Wo, 4096 * 1024)), [wva] "v"(wva), [wvb] "v"(wvb), [wl] "s"(wl), [go] "s"(tg.go), [tlo] "s"(tg.lo), [thi] "s"(tg.hi), [wr0] "v"(wr0), [wr1] "v"(wr1), [w] "s"(w),
                      [PZ] "s"(a.d4 & 255), [pf0] "v"(w < (a.d4 >> 8 & 7) ? 0xF0000000u : pf0), [pf1] "v"(3 - w < (a.d4 >> 12) ? 0xF0000000u : pf0 + 16384), [uo] "v"(uo), [uo2] "v"(uo + 65536), [ldw] "v"(ldw), [ldr] "v"(ldr) : CLOB_W);
            }
        }
        if (fail == 2) { BAR(); return true; }
        if (fail) ST4(1u, rr, F_ERR, 0);
        return false;
    }
    bool fin = true;
    if (bx == 3) {
        pm = producer<0>(a, rr, tg, br, 0, w, [&]() { if (ms) master(rr, tg, att, xnew); });
        if (pm < 2) qworker(a, rr, pm, br, w, QN0 + 1);
        fin = false;
    } else {
        const int h = (bx - 4) * 8 + (br & 7), kind = br >> 3;
        if (kind < 2) { pm = producer<2>(a, rr, tg, h, kind, w, []() {});  }
        else {
            if ((b >> 1) == 64) { if (ms) master(rr, tg, att, xnew); }
            const int idx = (br & 6) * 4 + (bx - 4) * 2 + (br & 1);
            if (kind == 2 && idx < 22) pm = producer<3>(a, rr, tg, idx >> 1, idx & 1, w, []() {});
            else { pm = gate(rr, tg, tg.go); if (kind == 3) { fin = false; if (pm < 2) qworker(a, rr, pm, h, w, 0); } }
        }
    }
    if (pm == 2 || pm == 3) { BAR(); return true; }
    if (pm > 1) return false;
    if (fin) finisher(a, rr, pm, w);
    return false;
}
__global__ void __launch_bounds__(256, 1) k_main(Args a) {
    extern __shared__ __attribute__((aligned(16))) unsigned char lds[];
    const int w = __builtin_amdgcn_readfirstlane(threadIdx.x >> 6);
    const unsigned t00 = 0;
    rsrc_t rr = mkr(a.ws, O_ALL);
    const unsigned xcc = __builtin_amdgcn_s_getreg(20 | (3 << 11)) & 7;
    const v2u xr = __builtin_amdgcn_raw_buffer_load_b64(rr, 0, F_X, 16);
    {
        u64 pc; unsigned len, keep;
        asm volatile("s_getpc_b64 %0\nkm_here%=:\n\ts_mov_b32 %1, km_mark-km_here%=\n\ts_max_u32 %1, %1, km_mark2-km_here%=" : "=s"(pc), "=s"(len) :: "scc");
        asm volatile("s_mov_b32 %0, m0\n\ts_mov_b32 m0, %3\n\ts_nop 0\n\tbuffer_load_dword %1, %2, 0 offen lds\n\ts_mov_b32 m0, %0"
                     : "=&s"(keep) : "v"((threadIdx.x * 32 + (blockIdx.x >> 3)) * 128), "s"(mkr((const void*)pc, len)), "s"(w * 32768 + 256) : "memory");
    }
    u64 X = 0;
    if (run<0>(a, rr, X, xr, xcc, w, t00)) run<1>(a, rr, X, v2u{0, 0}, xcc, w, t00);
}

extern "C" void kernel_launch(void* const* d_in, const int* in_sizes, int n_in, void* d_out, int out_size, void* d_ws, size_t ws_size, hipStream_t stream) {
    Args a;
    a.x = (const u16*)d_in[0]; a.mem0 = (const u16*)d_in[1]; a.Wq = (const u16*)d_in[2]; a.Wk = (const u16*)d_in[3]; a.Wv = (const u16*)d_in[4]; a.Wo = (const u16*)d_in[5];
    a.w1 = (const u16*)d_in[8]; a.b1 = (const u16*)d_in[9]; a.w2 = (const u16*)d_in[10]; a.b2 = (const u16*)d_in[11];
    a.out = (u16*)d_out;
    a.ws = (unsigned char*)(((size_t)d_ws + (2u << 20) - 1) & ~(size_t)((2u << 20) - 1));
    a.A = PA; a.Per = PPER; a.RT = 66; a.V = 3; a.V2 = 80 + (480 << 8); a.pad = 90; a.d1 = 22; a.d2 = 12 + (4 << 8); a.d3 = 264; a.d4 = 20 + (2 << 12); a.d5 = 266; a.d6 = (40 << 8) + (16 << 16);
    (void)hipFuncSetAttribute((const void*)k_main, hipFuncAttributeMaxDynamicSharedMemorySize, LDS_BYTES);
    k_main<<<256, 256, LDS_BYTES, stream>>>(a);
}
```
